```python
import torch

def _softmax(x, axis=-1):
    return torch.softmax(x, dim=axis)
def _gelu(x):
    return torch.nn.functional.gelu(x, approximate="tanh")
def _silu(x):
    return torch.nn.functional.silu(x)
def _sigmoid(x):
    return torch.sigmoid(x)
def _layernorm(x, axis=-1, eps=1e-5):
    m = x.mean(dim=axis, keepdim=True)
    v = x.var(dim=axis, keepdim=True, unbiased=False)
    return (x - m) / torch.sqrt(v + eps)
def _scatter_add(tgt, idx, src, axis=0):
    return tgt.clone().index_add_(axis, idx, src)
def _segment_sum(src, seg, n):
    out = torch.zeros((n,) + src.shape[1:], dtype=src.dtype)
    return out.index_add_(0, seg, src)
def _pad(x, pw):
    flat = tuple(v for pair in reversed(pw) for v in pair)
    return torch.nn.functional.pad(x, flat)

kernel_name = 'standalone__weight_only_int_squeezellm__prompt'

def setup_inputs():

    _w = (lambda a: (lambda t: t.to(torch.bfloat16) if t.is_floating_point() else t)(torch.as_tensor(a)))
    B, S, D, F = 1, 2048, 1024, 3584
    bits = 3 if (2 <= 3 <= 4) else 4
    L = 2 ** bits
    qw = torch.randint(0, L, (D, F)).to(torch.int64)
    lut = (lambda *s: torch.randn(*s, dtype=torch.bfloat16) * 0.05)(L, F)
    return {
        "x": _w((lambda *s: torch.randn(*s, dtype=torch.bfloat16) * 0.05)(B, S, D)),
        "qw": _w(qw),
        "lut": _w(lut),
    }

def reference(x, qw, lut):
    w = (lambda x, i, axis: torch.take_along_dim(x, i, dim=axis))(lut, qw, axis=0)
    w = (lambda x, dt: x.to(dt))(w, x.dtype)
    return torch.einsum('bsd,df->bsf', x, w)
```

```cpp
#include <hip/hip_runtime.h>
#include <hip/hip_bf16.h>
#include <stdint.h>

typedef __attribute__((ext_vector_type(8))) __bf16 bf16x8;
typedef __attribute__((ext_vector_type(4))) float f32x4;
typedef __attribute__((ext_vector_type(4))) unsigned u32x4;
typedef __attribute__((ext_vector_type(2))) unsigned u32x2;

namespace {

constexpr int M_ = 2048, K_ = 1024, N_ = 3584;
constexpr int BM = 256, BN = 112, BK = 64;
constexpr int NT = K_ / BK;
constexpr int A_STAGE = BM * BK * 2;
constexpr int B_STAGE = BN * BK * 2;
constexpr int LDS_B_OFF = 4 * A_STAGE;
constexpr int LDS_TOTAL = LDS_B_OFF + 2 * B_STAGE;
constexpr int OUT_STRIDE = 240;

#define WAIT_VM(n) asm volatile("s_waitcnt vmcnt(" #n ")" ::: "memory")
#define WAIT_LGKM0() asm volatile("s_waitcnt lgkmcnt(0)" ::: "memory")
#define BARRIER()                          \
    do {                                   \
        asm volatile("" ::: "memory");     \
        __builtin_amdgcn_s_barrier();      \
        asm volatile("" ::: "memory");     \
    } while (0)

__device__ __forceinline__ void glds16(unsigned voff, const void* sbase, unsigned lds_dst) {
    asm volatile(
        "s_mov_b32 m0, %2\n\t"
        "s_nop 0\n\t"
        "global_load_lds_dwordx4 %0, %1"
        :
        : "v"(voff), "s"(sbase), "s"(lds_dst)
        : "memory");
}

__device__ __forceinline__ void gload16(u32x4& dst, unsigned voff, const void* sbase) {
    asm volatile("global_load_dwordx4 %0, %1, %2" : "=v"(dst) : "v"(voff), "s"(sbase) : "memory");
}

__device__ __forceinline__ unsigned perm(unsigned hi, unsigned lo, unsigned sel) {
    return __builtin_amdgcn_perm(hi, lo, sel);
}

struct QSet {
    u32x4 r[8];
};

__device__ __forceinline__ void issue_q(QSet& q, const unsigned (&qoff)[8], const char* qbase) {
#pragma unroll
    for (int j = 0; j < 8; ++j) gload16(q.r[j], qoff[j], qbase);
}

__device__ __forceinline__ void pin_q(QSet& q) {
    asm volatile(""
                 : "+v"(q.r[0]), "+v"(q.r[1]), "+v"(q.r[2]), "+v"(q.r[3]), "+v"(q.r[4]), "+v"(q.r[5]),
                   "+v"(q.r[6]), "+v"(q.r[7])
                 :
                 : "memory");
}

__global__ __launch_bounds__(512, 2) void sqllm_gemm_kernel(const __hip_bfloat16* __restrict__ x,
                                                            const int* __restrict__ qw,
                                                            const __hip_bfloat16* __restrict__ lut,
                                                            __hip_bfloat16* __restrict__ out) {
    __shared__ __attribute__((aligned(1024))) char smem[LDS_TOTAL];
    const int tid = threadIdx.x;
    const int wid = __builtin_amdgcn_readfirstlane(tid >> 6);
    const int lane = tid & 63;
    const int b = blockIdx.x;
    const int xcd = b & 7, idx = b >> 3;
    const int ntile = xcd * 4 + (idx & 3);
    const int mtile = idx >> 2;
    const int m0 = mtile * BM, n0 = ntile * BN;
    const unsigned lds_base = (unsigned)(uintptr_t)(__attribute__((address_space(3))) char*)smem;

    if (wid >= 4) {
        const int p = wid - 4;
        const int nq_raw = lane & 31;
        const bool active = nq_raw < 28;
        const int nq = active ? nq_raw : 27;
        const int kg = 2 * p + (lane >> 5);

        unsigned qoff[8];
#pragma unroll
        for (int j = 0; j < 8; ++j) qoff[j] = (unsigned)(((kg * 8 + j) * N_ + nq * 4) * 4);
        const char* qbase0 = (const char*)(qw + n0);
        constexpr size_t QT = (size_t)BK * N_ * 4;

        unsigned xoff[2];
#pragma unroll
        for (int e = 0; e < 2; ++e) {
            const int f = (4 * e + (lane >> 4)) & 7;
            xoff[e] = (unsigned)((lane >> 3) * (K_ * 2) + (((lane & 7) ^ f) << 4));
        }
        const char* xbase0 = (const char*)(x + (size_t)(m0 + p * 64) * K_);
        const unsigned a_lds = lds_base + p * 8192;

        auto issue_a = [&](int t) {
#pragma unroll
            for (int i = 0; i < 8; ++i)
                glds16(xoff[i & 1], xbase0 + (size_t)i * 8 * K_ * 2 + t * (BK * 2),
                       a_lds + (t & 3) * A_STAGE + i * 1024);
        };

        u32x2 lraw[8];
        {
            const char* lb = (const char*)(lut + n0 + nq * 4);
#pragma unroll
            for (int c = 0; c < 8; ++c)
                asm volatile("global_load_dwordx2 %0, %1, off" : "=v"(lraw[c]) : "v"(lb + (size_t)c * N_ * 2) : "memory");
        }

        QSet q0, q1, q2, q3;
        issue_q(q0, qoff, qbase0 + 0 * QT);
        issue_a(0);
        issue_q(q1, qoff, qbase0 + 1 * QT);
        issue_a(1);
        issue_q(q2, qoff, qbase0 + 2 * QT);
        issue_a(2);

        asm volatile("s_waitcnt vmcnt(48)"
                     : "+v"(lraw[0]), "+v"(lraw[1]), "+v"(lraw[2]), "+v"(lraw[3]), "+v"(lraw[4]), "+v"(lraw[5]),
                       "+v"(lraw[6]), "+v"(lraw[7])
                     :
                     : "memory");
        issue_q(q3, qoff, qbase0 + 3 * QT);
        unsigned LO[4][2], HI[4][2];
#pragma unroll
        for (int c = 0; c < 4; ++c) {
#pragma unroll
            for (int h = 0; h < 2; ++h) {
                const unsigned e0 = lraw[4 * h + 0][c >> 1], e1 = lraw[4 * h + 1][c >> 1];
                const unsigned e2 = lraw[4 * h + 2][c >> 1], e3 = lraw[4 * h + 3][c >> 1];
                const unsigned sel01 = (c & 1) ? 0x07030602u : 0x05010400u;
                const unsigned t01 = perm(e1, e0, sel01);
                const unsigned t23 = perm(e3, e2, sel01);
                LO[c][h] = perm(t23, t01, 0x05040100u);
                HI[c][h] = perm(t23, t01, 0x07060302u);
            }
        }

        unsigned baddr[4];
#pragma unroll
        for (int c = 0; c < 4; ++c) {
            const int n = nq * 4 + c;
            const int f = ((n >> 1) & 7) ^ ((n >> 4) & 1);
            baddr[c] = (unsigned)(LDS_B_OFF + n * 128 + ((kg ^ f) << 4));
        }

        auto dequant = [&](QSet& q, int t) {
#pragma unroll
            for (int c = 0; c < 4; ++c) {
                const unsigned s0 = q.r[0][c] | (q.r[1][c] << 8) | (q.r[2][c] << 16) | (q.r[3][c] << 24);
                const unsigned s1 = q.r[4][c] | (q.r[5][c] << 8) | (q.r[6][c] << 16) | (q.r[7][c] << 24);
                const unsigned lo0 = perm(LO[c][1], LO[c][0], s0), hi0 = perm(HI[c][1], HI[c][0], s0);
                const unsigned lo1 = perm(LO[c][1], LO[c][0], s1), hi1 = perm(HI[c][1], HI[c][0], s1);
                u32x4 w;
                w[0] = perm(hi0, lo0, 0x05010400u);
                w[1] = perm(hi0, lo0, 0x07030602u);
                w[2] = perm(hi1, lo1, 0x05010400u);
                w[3] = perm(hi1, lo1, 0x07030602u);
                if (active) *(u32x4*)(smem + baddr[c] + (t & 1) * B_STAGE) = w;
            }
        };

#define PROD_STEP(T, QS, WQ, WA)                                   \
    do {                                                           \
        if ((T) + 3 >= 3 && (T) + 3 < NT) issue_a((T) + 3);        \
        WAIT_VM(WQ);                                               \
        pin_q(QS);                                                 \
        dequant(QS, (T) + 1);                                      \
        pin_q(QS);                                                 \
        if ((T) + 5 < NT) issue_q(QS, qoff, qbase0 + ((T) + 5) * QT); \
        WAIT_VM(WA);                                               \
        WAIT_LGKM0();                                              \
        BARRIER();                                                 \
    } while (0)

        PROD_STEP(-1, q0, 48, 48);
        PROD_STEP(0, q1, 48, 48);
        PROD_STEP(1, q2, 48, 48);
        PROD_STEP(2, q3, 48, 40);
        PROD_STEP(3, q0, 48, 40);
        PROD_STEP(4, q1, 48, 40);
        PROD_STEP(5, q2, 48, 40);
        PROD_STEP(6, q3, 48, 40);
        PROD_STEP(7, q0, 48, 40);
        PROD_STEP(8, q1, 48, 40);
        PROD_STEP(9, q2, 48, 40);
        PROD_STEP(10, q3, 48, 40);
        PROD_STEP(11, q0, 48, 32);
        PROD_STEP(12, q1, 48, 24);
        PROD_STEP(13, q2, 48, 8);
        PROD_STEP(14, q3, 48, 0);
#undef PROD_STEP
    } else {
        __builtin_amdgcn_s_setprio(2);
        const int r = lane & 15, qd = lane >> 4;
        f32x4 acc[4][7];
#pragma unroll
        for (int i = 0; i < 4; ++i)
#pragma unroll
            for (int j = 0; j < 7; ++j) acc[i][j] = (f32x4){0.f, 0.f, 0.f, 0.f};

        const int fa = (r >> 1) & 7;
        const unsigned xa0 = (unsigned)((wid * 64 + r) * 128 + ((qd ^ fa) << 4));
        const unsigned xa1 = xa0 ^ 64u;
        const unsigned wa00 = (unsigned)(LDS_B_OFF + r * 128 + ((qd ^ fa) << 4));
        const unsigned wa01 = wa00 ^ 16u;
        const unsigned wa10 = wa00 ^ 64u;
        const unsigned wa11 = wa00 ^ 80u;

        bf16x8 xf0[4], wf0[7], xf1[4], wf1[7];

        auto load_set = [&](bf16x8(&xf)[4], bf16x8(&wf)[7], unsigned xa, unsigned wae, unsigned wao) {
#pragma unroll
            for (int j = 0; j < 7; ++j)
                wf[j] = *(const bf16x8*)(smem + ((j & 1) ? wao : wae) + j * 2048);
#pragma unroll
            for (int i = 0; i < 4; ++i) xf[i] = *(const bf16x8*)(smem + xa + i * 2048);
        };
        auto mma_set = [&](bf16x8(&xf)[4], bf16x8(&wf)[7]) {
#pragma unroll
            for (int i = 0; i < 4; ++i)
#pragma unroll
                for (int j = 0; j < 7; ++j)
                    acc[i][j] = __builtin_amdgcn_mfma_f32_16x16x32_bf16(wf[j], xf[i], acc[i][j], 0, 0, 0);
        };
        auto interleave = [&]() {
#pragma unroll
            for (int i = 0; i < 11; ++i) {
                __builtin_amdgcn_sched_group_barrier(0x008, 1, 0);
                __builtin_amdgcn_sched_group_barrier(0x100, 1, 0);
            }
            __builtin_amdgcn_sched_group_barrier(0x008, 17, 0);
        };

        BARRIER();
        load_set(xf0, wf0, xa0, wa00, wa01);
#pragma unroll 1
        for (int t = 0; t < NT - 1; ++t) {
            const unsigned aoff = (unsigned)(t & 3) * A_STAGE;
            const unsigned boff = (unsigned)(t & 1) * B_STAGE;
            load_set(xf1, wf1, xa1 + aoff, wa10 + boff, wa11 + boff);
            mma_set(xf0, wf0);
            interleave();
            __builtin_amdgcn_sched_barrier(0);
            WAIT_LGKM0();
            BARRIER();
            const unsigned aoff2 = (unsigned)((t + 1) & 3) * A_STAGE;
            const unsigned boff2 = (unsigned)((t + 1) & 1) * B_STAGE;
            load_set(xf0, wf0, xa0 + aoff2, wa00 + boff2, wa01 + boff2);
            mma_set(xf1, wf1);
            interleave();
            __builtin_amdgcn_sched_barrier(0);
        }
        {
            constexpr unsigned aoff = (unsigned)((NT - 1) & 3) * A_STAGE;
            constexpr unsigned boff = (unsigned)((NT - 1) & 1) * B_STAGE;
            load_set(xf1, wf1, xa1 + aoff, wa10 + boff, wa11 + boff);
            mma_set(xf0, wf0);
            interleave();
            __builtin_amdgcn_sched_barrier(0);
            mma_set(xf1, wf1);
        }

#pragma unroll
        for (int i = 0; i < 4; ++i) {
#pragma unroll
            for (int j = 0; j < 7; ++j) {
                typedef __attribute__((ext_vector_type(4))) __bf16 bf16x4;
                bf16x4 v;
                v[0] = (__bf16)acc[i][j][0];
                v[1] = (__bf16)acc[i][j][1];
                v[2] = (__bf16)acc[i][j][2];
                v[3] = (__bf16)acc[i][j][3];
                *(bf16x4*)(smem + (wid * 64 + i * 16 + r) * OUT_STRIDE + j * 32 + qd * 8) = v;
            }
        }
        __builtin_amdgcn_s_setprio(0);
    }

    WAIT_LGKM0();
    BARRIER();

#pragma unroll
    for (int it = 0; it < 7; ++it) {
        const int c = it * 512 + tid;
        const int row = c / 14, ch = c - row * 14;
        const u32x4 v = *(const u32x4*)(smem + row * OUT_STRIDE + ch * 16);
        *(u32x4*)((char*)(out + (size_t)(m0 + row) * N_ + n0) + ch * 16) = v;
    }
}

}

extern "C" void kernel_launch(void* const* d_in, const int* in_sizes, int n_in, void* d_out, int out_size,
                              void* d_ws, size_t ws_size, hipStream_t stream) {
    const __hip_bfloat16* x = (const __hip_bfloat16*)d_in[0];
    const int* qw = (const int*)d_in[1];
    const __hip_bfloat16* lut = (const __hip_bfloat16*)d_in[2];
    __hip_bfloat16* out = (__hip_bfloat16*)d_out;
    sqllm_gemm_kernel<<<dim3(256), dim3(512), 0, stream>>>(x, qw, lut, out);
}
```

```cpp
#include <hip/hip_runtime.h>
#include <hip/hip_bf16.h>
#include <stdint.h>

typedef __attribute__((ext_vector_type(8))) __bf16 bf16x8;
typedef __attribute__((ext_vector_type(4))) __bf16 bf16x4;
typedef __attribute__((ext_vector_type(4))) float f32x4;
typedef __attribute__((ext_vector_type(4))) unsigned u32x4;
typedef __attribute__((ext_vector_type(2))) unsigned u32x2;

namespace {

constexpr int M_ = 2048, K_ = 1024, N_ = 3584;
constexpr int BM = 128, BN = 224;
constexpr int STAGE = 128 * 128;
constexpr int ST_BYTES = 2 * STAGE;
constexpr int NST = K_ / 128;
constexpr int LDS_TOTAL = 4 * ST_BYTES;
constexpr int OUT_STRIDE = 464;
constexpr size_t WS_PACK_OFF = 1 << 20;
constexpr size_t WS_LUTP_OFF = 4 << 20;

#define STAMP(role, slot) do {} while (0)
#define STAMPF(role, slot) do {} while (0)
#define WAIT_LGKM0() asm volatile("s_waitcnt lgkmcnt(0)" ::: "memory")
#define BARRIER()                          \
    do {                                   \
        asm volatile("" ::: "memory");     \
        __builtin_amdgcn_s_barrier();      \
        asm volatile("" ::: "memory");     \
    } while (0)
#define SCHED0() __builtin_amdgcn_sched_barrier(0)

__device__ __forceinline__ unsigned perm(unsigned hi, unsigned lo, unsigned sel) {
    return __builtin_amdgcn_perm(hi, lo, sel);
}

__global__ __launch_bounds__(256) void sqllm_pack_kernel(const int* __restrict__ qw, const __hip_bfloat16* __restrict__ lut,
                                                         char* __restrict__ pk, u32x4* __restrict__ lutp) {
    __shared__ __attribute__((aligned(16))) unsigned tr[4][2][2][112];
    const int lane = threadIdx.x & 63, wid = threadIdx.x >> 6;
    const int m = blockIdx.x & 7, n = blockIdx.x >> 3;
    const int nq_raw = lane & 31, ql = lane >> 5;
    const int nq = nq_raw < 28 ? nq_raw : 27;
    u32x4 q[2][8];
#pragma unroll
    for (int rd = 0; rd < 2; ++rd) {
        const int kg = ((rd * 4 + wid) * 4 + (m >> 1)) * 4 + 2 * (m & 1) + ql;
        const int* src = qw + (size_t)(kg * 8) * N_ + n * 112 + nq * 4;
#pragma unroll
        for (int j = 0; j < 8; ++j) q[rd][j] = __builtin_nontemporal_load((const u32x4*)(src + (size_t)j * N_));
    }
    if (m == 0 && threadIdx.x < 28) {
        u32x2 lraw[8];
#pragma unroll
        for (int c = 0; c < 8; ++c) lraw[c] = *(const u32x2*)(lut + (size_t)c * N_ + n * 112 + nq * 4);
#pragma unroll
        for (int c = 0; c < 4; ++c) {
            u32x4 o;
#pragma unroll
            for (int h = 0; h < 2; ++h) {
                const unsigned e0 = lraw[4 * h + 0][c >> 1], e1 = lraw[4 * h + 1][c >> 1];
                const unsigned e2 = lraw[4 * h + 2][c >> 1], e3 = lraw[4 * h + 3][c >> 1];
                const unsigned sel01 = (c & 1) ? 0x07030602u : 0x05010400u;
                const unsigned t01 = perm(e1, e0, sel01);
                const unsigned t23 = perm(e3, e2, sel01);
                o[h] = perm(t23, t01, 0x05040100u);
                o[2 + h] = perm(t23, t01, 0x07060302u);
            }
            lutp[n * 112 + nq * 4 + c] = o;
        }
    }
#pragma unroll
    for (int rd = 0; rd < 2; ++rd) {
        u32x4 p;
#pragma unroll
        for (int c = 0; c < 4; ++c) {
            const unsigned s0 = q[rd][0][c] | (q[rd][1][c] << 8) | (q[rd][2][c] << 16) | (q[rd][3][c] << 24);
            const unsigned s1 = q[rd][4][c] | (q[rd][5][c] << 8) | (q[rd][6][c] << 16) | (q[rd][7][c] << 24);
            p[c] = s0 | (s1 << 4);
        }
        *(u32x4*)&tr[wid][rd][ql][nq * 4] = p;
    }
    __builtin_amdgcn_wave_barrier();
    asm volatile("s_waitcnt lgkmcnt(0)" ::: "memory");
#pragma unroll
    for (int rd = 0; rd < 2; ++rd) {
        const int hh = lane >> 5, q2 = (lane >> 4) & 1, r = lane & 15;
        const unsigned* t = &tr[wid][rd][q2][0];
        u32x4 o;
        o[0] = t[hh * 64 + r];
        o[1] = t[hh * 64 + 16 + r];
        o[2] = t[hh * 64 + 32 + r];
        o[3] = t[48 + r];
        const int ks = (rd * 4 + wid) * 4 + (m >> 1);
        *(u32x4*)(pk + (size_t)(((n * 2 + hh) * 32 + ks) * 64 + (2 * (m & 1) + q2) * 16 + r) * 16) = o;
    }
}

__device__ __forceinline__ void load_codes(u32x4& cs, unsigned voff, const char* sbase) {
    asm volatile("global_load_dwordx4 %0, %1, %2" : "+v"(cs) : "v"(voff), "s"(sbase) : "memory");
}
template <int N, int NTAIL>
__device__ __forceinline__ void wait_codes_t(u32x4& cs, int u, int lim) {
    asm volatile(
        "s_cmp_gt_i32 %1, %2\n\t"
        "s_cbranch_scc1 .Ltail%=\n\t"
        "s_waitcnt vmcnt(%3)\n\t"
        "s_branch .Lend%=\n"
        ".Ltail%=:\n\t"
        "s_waitcnt vmcnt(%4)\n"
        ".Lend%=:"
        : "+v"(cs)
        : "s"(u), "s"(lim), "n"(N), "n"(NTAIL)
        : "memory", "scc");
}
__device__ __forceinline__ void issue_x1(unsigned xo, const char* xbase, unsigned lds_dst, int u, int lim) {
    asm volatile(
        "s_cmp_gt_i32 %3, %4\n\t"
        "s_cbranch_scc1 .Lskip%=\n\t"
        "s_mov_b32 m0, %2\n\t"
        "s_nop 0\n\t"
        "global_load_lds_dwordx4 %0, %1\n"
        ".Lskip%=:"
        :
        : "v"(xo), "s"(xbase), "s"(lds_dst), "s"(u), "s"(lim)
        : "memory", "scc");
}
__device__ __forceinline__ void barrier_if(int sel, int val) {
    asm volatile(
        "s_cmp_lg_u32 %0, %1\n\t"
        "s_cbranch_scc1 .Lskip%=\n\t"
        "s_barrier\n"
        ".Lskip%=:"
        :
        : "s"(sel), "n"(val)
        : "memory", "scc");
}

__device__ __forceinline__ bf16x8 dequant(unsigned p, const u32x4& L) {
    const unsigned s0 = p & 0x07070707u;
    const unsigned s1 = (p >> 4) & 0x07070707u;
    const unsigned lo0 = perm(L[1], L[0], s0), hi0 = perm(L[3], L[2], s0);
    const unsigned lo1 = perm(L[1], L[0], s1), hi1 = perm(L[3], L[2], s1);
    u32x4 w;
    w[0] = perm(hi0, lo0, 0x05010400u);
    w[1] = perm(hi0, lo0, 0x07030602u);
    w[2] = perm(hi1, lo1, 0x05010400u);
    w[3] = perm(hi1, lo1, 0x07030602u);
    return __builtin_bit_cast(bf16x8, w);
}

__global__ __launch_bounds__(512, 2) void sqllm_gemm_kernel(const __hip_bfloat16* __restrict__ x,
                                                            const char* __restrict__ pk,
                                                            const char* __restrict__ lutp,
                                                            __hip_bfloat16* __restrict__ out, unsigned long long* stamps) {
    __shared__ __attribute__((aligned(1024))) char smem[LDS_TOTAL];
    const int tid = threadIdx.x;
    const int wid = __builtin_amdgcn_readfirstlane(tid >> 6);
    const int lane = tid & 63;
    const int xcd = blockIdx.x & 7, idx = blockIdx.x >> 3;
    const int mtile = (xcd >> 1) * 4 + (idx & 3), ntile = (xcd & 1) * 8 + (idx >> 2);
    const int m0 = mtile * BM, n0 = ntile * BN;
    const unsigned lds_base = (unsigned)(uintptr_t)(__attribute__((address_space(3))) char*)smem;

    const int kh = wid & 1, ch = (wid >> 1) & 1, hh = wid >> 2;
    const int r = lane & 15, qd = lane >> 4;

    unsigned xo0, xo1;
    {
        const int f0 = (lane >> 4) & 7, f1 = (4 + (lane >> 4)) & 7;
        xo0 = (unsigned)((lane >> 3) * (K_ * 2) + (((lane & 7) ^ f0) << 4));
        xo1 = (unsigned)((lane >> 3) * (K_ * 2) + (((lane & 7) ^ f1) << 4) + 8 * K_ * 2);
    }
    const char* xb = (const char*)(x + (size_t)(m0 + wid * 16) * K_);
    const unsigned x_lds = lds_base + wid * 2048;

    const int cb = n0 + ch * 112 + hh * 64, cs = n0 + ch * 112 + 48;
    const unsigned coff = (unsigned)(lane * 16);
    const char* pb = pk + (size_t)((((ntile * 2 + ch) * 2 + hh) * 32 + kh) * 1024);
    constexpr size_t CSTEP = 2048;

    u32x4 L[4];
    u32x4 c0 = {}, c1 = {}, cinit = {};
    {
        const unsigned lo_ = (unsigned)((cb + r) * 16), lo2 = (unsigned)((cs + r) * 16);
        asm volatile(
            "global_load_dwordx4 %0, %4, %6\n\t"
            "global_load_dwordx4 %1, %4, %6 offset:256\n\t"
            "global_load_dwordx4 %2, %4, %6 offset:512\n\t"
            "global_load_dwordx4 %3, %5, %6"
            : "=&v"(L[0]), "=&v"(L[1]), "=&v"(L[2]), "=&v"(L[3])
            : "v"(lo_), "v"(lo2), "s"(lutp)
            : "memory");
    }
    load_codes(cinit, coff, pb + 0 * CSTEP);
#pragma unroll
    for (int st = 0; st < 2; ++st) {
        issue_x1(xo0, xb + st * 128, x_lds + st * STAGE, 0, 1);
        issue_x1(xo1, xb + st * 128, x_lds + st * STAGE + 1024, 0, 1);
    }
    load_codes(c1, coff, pb + 1 * CSTEP);
#pragma unroll
    for (int st = 2; st < 4; ++st) {
        issue_x1(xo0, xb + st * 128, x_lds + st * STAGE, 0, 1);
        issue_x1(xo1, xb + st * 128, x_lds + st * STAGE + 1024, 0, 1);
    }
    load_codes(c0, coff, pb + 2 * CSTEP);

    f32x4 acc[8][4];
#pragma unroll
    for (int i = 0; i < 8; ++i)
#pragma unroll
        for (int j = 0; j < 4; ++j)
            if (i < 4 || j < 3) {
                acc[i][j] = (f32x4){0.f, 0.f, 0.f, 0.f};
                asm volatile("" : "+v"(acc[i][j]));
            }

    const unsigned xa = (unsigned)(r * 128 + (((4 * kh + qd) ^ ((r >> 1) & 7)) << 4));
    const unsigned rbx = (unsigned)((4 * hh + 2 * kh) * 2048);
    unsigned xb4[4];
#pragma unroll
    for (int i2 = 0; i2 < 4; ++i2) xb4[i2] = xa + ((unsigned)(i2 << 12) ^ rbx);

    asm volatile("s_waitcnt vmcnt(10)" : "+v"(L[0]), "+v"(L[1]), "+v"(L[2]), "+v"(L[3]), "+v"(cinit) : : "memory");
    STAMP(0, 2);
    bf16x8 wA[4], wB[4], xf[8];
#pragma unroll
    for (int j = 0; j < 4; ++j) wA[j] = dequant(cinit[j], L[j]);

    asm volatile("s_waitcnt vmcnt(6)" ::: "memory");
    BARRIER();
    STAMP(0, 3);
#pragma unroll
    for (int i = 0; i < 6; ++i) xf[i] = *(const bf16x8*)(smem + xb4[i >> 1] + (i & 1) * 2048);

    auto mrow = [&](int i, const bf16x8(&w)[4]) {
#pragma unroll
        for (int j = 0; j < 4; ++j)
            if (i < 4 || j < 3) acc[i][j] = __builtin_amdgcn_mfma_f32_16x16x32_bf16(w[j], xf[i], acc[i][j], 0, 0, 0);
    };
    auto region = [&](int i, const bf16x8(&w)[4], unsigned ra, bf16x8& wn, unsigned code, const u32x4& Lj) {
        xf[(i + 6) & 7] = *(const bf16x8*)(smem + ra);
        xf[(i + 7) & 7] = *(const bf16x8*)(smem + ra + 2048);
        mrow(i, w);
        mrow(i + 1, w);
        wn = dequant(code, Lj);
        __builtin_amdgcn_sched_group_barrier(0x008, 1, 0);
        __builtin_amdgcn_sched_group_barrier(0x100, 1, 0);
        __builtin_amdgcn_sched_group_barrier(0x002, 1, 0);
        __builtin_amdgcn_sched_group_barrier(0x008, 1, 0);
        __builtin_amdgcn_sched_group_barrier(0x100, 1, 0);
        __builtin_amdgcn_sched_group_barrier(0x002, 1, 0);
#pragma unroll
        for (int j = 0; j < (i < 4 ? 6 : 4); ++j) {
            __builtin_amdgcn_sched_group_barrier(0x008, 1, 0);
            __builtin_amdgcn_sched_group_barrier(0x002, 2, 0);
        }
        SCHED0();
    };
#define LADDER 1
#define PRIO(p) do { if (LADDER) __builtin_amdgcn_s_setprio(p); } while (0)

    unsigned a0 = xb4[0], a1 = xb4[1], a2 = xb4[2], a3 = xb4[3];
    PRIO(3);
#pragma unroll 1
    for (int u = 0; u < NST; ++u) {
        const char* xn = xb + (u + 2) * 256;
        const unsigned xl = x_lds + ((u + 2) & 3) * ST_BYTES;
        const unsigned nx = (unsigned)((u + 1) & 3) * ST_BYTES;
        wait_codes_t<3, 1>(c1, u, 6);
        SCHED0();
        region(0, wA, a3, wB[0], c1[0], L[0]);
        issue_x1(xo0, xn, xl, u, 5);
        PRIO(2);
        region(2, wA, a0 + STAGE, wB[1], c1[1], L[1]);
        issue_x1(xo1, xn, xl + 1024, u, 5);
        a0 = xb4[0] + nx;
        region(4, wA, a1 + STAGE, wB[2], c1[2], L[2]);
        PRIO(1);
        a1 = xb4[1] + nx;
        region(6, wA, a2 + STAGE, wB[3], c1[3], L[3]);
        load_codes(c1, coff, pb + (size_t)((2 * u + 3) & 15) * CSTEP);
        wait_codes_t<3, 1>(c0, u, 5);
        PRIO(0);
        SCHED0();
        a2 = xb4[2] + nx;
        region(0, wB, a3 + STAGE, wA[0], c0[0], L[0]);
        BARRIER();
        PRIO(3);
        STAMPF(0, 4 + u);
        SCHED0();
        a3 = xb4[3] + nx;
        region(2, wB, a0, wA[1], c0[1], L[1]);
        issue_x1(xo0, xn + 128, xl + STAGE, u, 5);
        region(4, wB, a1, wA[2], c0[2], L[2]);
        issue_x1(xo1, xn + 128, xl + STAGE + 1024, u, 5);
        region(6, wB, a2, wA[3], c0[3], L[3]);
        load_codes(c0, coff, pb + (size_t)((2 * u + 4) & 15) * CSTEP);
    }
    PRIO(0);
    STAMP(0, 20);

    asm volatile("s_waitcnt vmcnt(0) lgkmcnt(0)" : "+v"(c0), "+v"(c1) :: "memory");
    BARRIER();
    {
        char* xs = smem + wid * 14336 + lane * 16;
        int e = 0;
#pragma unroll
        for (int i = 0; i < 8; ++i)
            if (i & 2) {
#pragma unroll
                for (int j = 0; j < 4; ++j)
                    if (i < 4 || j < 3) { *(f32x4*)(xs + e * 1024) = acc[i][j]; ++e; }
            }
    }
    WAIT_LGKM0();
    BARRIER();
    STAMP(0, 21);
    {
        const char* xr = smem + (wid ^ 1) * 14336 + lane * 16;
        int e = 0;
#pragma unroll
        for (int i = 0; i < 8; ++i)
            if (!(i & 2)) {
#pragma unroll
                for (int j = 0; j < 4; ++j)
                    if (i < 4 || j < 3) { acc[i][j] += *(const f32x4*)(xr + e * 1024); ++e; }
            }
    }
    WAIT_LGKM0();
    BARRIER();
    {
#pragma unroll
        for (int i = 0; i < 8; ++i)
            if (!(i & 2)) {
                const int rb = i ^ (4 * hh) ^ (2 * kh);
#pragma unroll
                for (int j = 0; j < 4; ++j)
                    if (i < 4 || j < 3) {
                        bf16x4 v;
                        v[0] = (__bf16)acc[i][j][0];
                        v[1] = (__bf16)acc[i][j][1];
                        v[2] = (__bf16)acc[i][j][2];
                        v[3] = (__bf16)acc[i][j][3];
                        const int col = ch * 112 + (j < 3 ? hh * 64 + 16 * j : 48) + qd * 4;
                        *(bf16x4*)(smem + (rb * 16 + r) * OUT_STRIDE + col * 2) = v;
                    }
            }
    }
    WAIT_LGKM0();
    BARRIER();
    STAMP(0, 22);
    {
        const int rr = lane / 28, cc = lane - rr * 28;
        const int row = wid * 16 + rr;
        const char* src = smem + row * OUT_STRIDE + cc * 16;
        char* dst = (char*)(out + (size_t)(m0 + row) * N_ + n0) + cc * 16;
        if (lane < 56) {
#pragma unroll
            for (int it = 0; it < 8; ++it) {
                const u32x4 v = *(const u32x4*)(src + it * 2 * OUT_STRIDE);
                *(u32x4*)(dst + (size_t)it * 2 * N_ * 2) = v;
            }
        }
    }
}

}

extern "C" void kernel_launch(void* const* d_in, const int* in_sizes, int n_in, void* d_out, int out_size,
                              void* d_ws, size_t ws_size, hipStream_t stream) {
    const __hip_bfloat16* x = (const __hip_bfloat16*)d_in[0];
    const int* qw = (const int*)d_in[1];
    const __hip_bfloat16* lut = (const __hip_bfloat16*)d_in[2];
    __hip_bfloat16* out = (__hip_bfloat16*)d_out;
    char* pk = (char*)d_ws + WS_PACK_OFF;
    char* lp = (char*)d_ws + WS_LUTP_OFF;
    sqllm_pack_kernel<<<dim3(256), dim3(256), 0, stream>>>(qw, lut, pk, (u32x4*)lp);
    sqllm_gemm_kernel<<<dim3(256), dim3(512), 0, stream>>>(x, pk, lp, out, (unsigned long long*)d_ws);
}
```

```cpp
#define NPRE 8
#define STPOL " nt"
#include <hip/hip_runtime.h>
#include <hip/hip_bf16.h>
#include <stdint.h>

typedef __attribute__((ext_vector_type(8))) __bf16 bf16x8;
typedef __attribute__((ext_vector_type(4))) __bf16 bf16x4;
typedef __attribute__((ext_vector_type(4))) float f32x4;
typedef __attribute__((ext_vector_type(4))) unsigned u32x4;
typedef __attribute__((ext_vector_type(2))) unsigned u32x2;
typedef unsigned long long u64;

namespace {

constexpr int M_ = 2048, K_ = 1024, N_ = 3584;
constexpr int BM = 128, BN = 224;
constexpr int STAGE = 128 * 128;
constexpr int ST_BYTES = 2 * STAGE;
constexpr int NST = K_ / 128;
constexpr int LDS_RING = 4 * ST_BYTES;
constexpr int LDS_LUT = LDS_RING;
constexpr int LDS_TICKET = LDS_RING + 4096;
constexpr int LDS_RAWQ2 = LDS_RING + 4096 + 1024;
constexpr int LDS_TOTAL = LDS_RAWQ2 + 2 * 8192;
constexpr int LDS_RAWQ = 2 * ST_BYTES;
constexpr unsigned TAGM = 0x88888888u;
constexpr int OUT_STRIDE = 464;
constexpr size_t WS_PACK_OFF = 1 << 20;
constexpr size_t WS_CTR_OFF = 4 << 20;

#define STAMP(role, slot) do {} while (0)
#define STAMPF(role, slot) do {} while (0)
#define WAIT_LGKM0() asm volatile("s_waitcnt lgkmcnt(0)" ::: "memory")
#define WAIT_VM(n) asm volatile("s_waitcnt vmcnt(" #n ")" ::: "memory")
#define BARRIER()                          \
    do {                                   \
        asm volatile("" ::: "memory");     \
        __builtin_amdgcn_s_barrier();      \
        asm volatile("" ::: "memory");     \
    } while (0)
#define SCHED0() __builtin_amdgcn_sched_barrier(0)

__device__ __forceinline__ unsigned perm(unsigned hi, unsigned lo, unsigned sel) {
    return __builtin_amdgcn_perm(hi, lo, sel);
}

#define C0REG "v[216:219]"
#define C0D3 "v219"
#define C1REG "v[220:223]"
#define C1D3 "v223"
#define LOAD_CODES(REG, cs, voff, sbase) \
    asm volatile("global_load_dwordx4 " REG ", %1, %2 sc1" : "+{" REG "}"(cs) : "v"(voff), "s"(sbase) : "memory")
#define WAIT_VERIFY(REG, D3, cs, N0, N1, N2, u, lim, a, b, e, voff, src)                        \
    do {                                                                                        \
        unsigned tmp_;                                                                          \
        int cnt_;                                                                               \
        asm volatile(                                                                           \
            "s_cmp_gt_i32 %3, %4\n\t"                                                           \
            "s_cbranch_scc1 .La%=\n\t"                                                          \
            "s_cmp_eq_u32 %5, %6\n\t"                                                           \
            "s_cbranch_scc1 .Lb%=\n\t"                                                          \
            "s_waitcnt vmcnt(" #N0 ")\n\t"                                                      \
            "s_branch .Lchk%=\n"                                                                \
            ".La%=:\n\t"                                                                        \
            "s_waitcnt vmcnt(" #N1 ")\n"                                                        \
            ".Lb%=:\n\t"                                                                        \
            "s_waitcnt vmcnt(" #N2 ")\n"                                                        \
            ".Lchk%=:\n\t"                                                                      \
            "v_bitop3_b32 %1, " D3 ", %7, %10 bitop3:0x28\n\t"                                  \
            "v_cmp_ne_u32 vcc, 0, %1\n\t"                                                       \
            "s_cbranch_vccz .Lok%=\n\t"                                                         \
            "s_movk_i32 %2, 0x7fff\n"                                                           \
            ".Lretry%=:\n\t"                                                                    \
            "s_sleep 1\n\t"                                                                     \
            "global_load_dwordx4 " REG ", %8, %9 sc1\n\t"                                       \
            "s_waitcnt vmcnt(0)\n\t"                                                            \
            "v_bitop3_b32 %1, " D3 ", %7, %10 bitop3:0x28\n\t"                                  \
            "v_cmp_ne_u32 vcc, 0, %1\n\t"                                                       \
            "s_cbranch_vccz .Lok%=\n\t"                                                         \
            "s_sub_u32 %2, %2, 1\n\t"                                                           \
            "s_cbranch_scc0 .Lretry%=\n"                                                        \
            ".Lok%=:"                                                                           \
            : "+{" REG "}"(cs), "=&v"(tmp_), "=&s"(cnt_)                                        \
            : "s"(u), "s"(lim), "s"(a), "s"(b), "v"(e), "v"(voff), "s"(src), "s"(TAGM)          \
            : "memory", "scc", "vcc");                                                          \
    } while (0)
__device__ __forceinline__ void glds(unsigned voff, const char* sbase, unsigned lds_dst) {
    asm volatile(
        "s_mov_b32 m0, %2\n\t"
        "s_nop 0\n\t"
        "global_load_lds_dwordx4 %0, %1"
        :
        : "v"(voff), "s"(sbase), "s"(lds_dst)
        : "memory");
}
__device__ __forceinline__ void glds_nt(unsigned voff, const char* sbase, unsigned lds_dst) {
    asm volatile(
        "s_mov_b32 m0, %2\n\t"
        "s_nop 0\n\t"
        "global_load_lds_dwordx4 %0, %1 nt"
        :
        : "v"(voff), "s"(sbase), "s"(lds_dst)
        : "memory");
}
#define XPOL ""
__device__ __forceinline__ void issue_x1(unsigned xo, const char* xbase, unsigned lds_dst, int u, int lim) {
    asm volatile(
        "s_cmp_gt_i32 %3, %4\n\t"
        "s_cbranch_scc1 .Lskip%=\n\t"
        "s_mov_b32 m0, %2\n\t"
        "s_nop 0\n\t"
        "global_load_lds_dwordx4 %0, %1" XPOL "\n"
        ".Lskip%=:"
        :
        : "v"(xo), "s"(xbase), "s"(lds_dst), "s"(u), "s"(lim)
        : "memory", "scc");
}

__device__ __forceinline__ bf16x8 lookup(unsigned s0, unsigned s1, const u32x4& L) {
    const unsigned lo0 = perm(L[1], L[0], s0), hi0 = perm(L[3], L[2], s0);
    const unsigned lo1 = perm(L[1], L[0], s1), hi1 = perm(L[3], L[2], s1);
    u32x4 w;
    w[0] = perm(hi0, lo0, 0x05010400u);
    w[1] = perm(hi0, lo0, 0x07030602u);
    w[2] = perm(hi1, lo1, 0x05010400u);
    w[3] = perm(hi1, lo1, 0x07030602u);
    return __builtin_bit_cast(bf16x8, w);
}
__device__ __forceinline__ unsigned spread8(unsigned b) {
    unsigned x = b & 0xffu;
    x = (x | (x << 12)) & 0x000F000Fu;
    x = (x | (x << 6)) & 0x03030303u;
    x = (x | (x << 3)) & 0x11111111u;
    return x << 3;
}
__device__ __forceinline__ unsigned gather8(unsigned c) {
    unsigned x = (c >> 3) & 0x11111111u;
    x = (x | (x >> 3)) & 0x03030303u;
    x = (x | (x >> 6)) & 0x000F000Fu;
    x = (x | (x >> 12)) & 0xFFu;
    return x;
}
__device__ __forceinline__ unsigned tag_b3(unsigned t) { return ((t & 1u) << 1) | 4u | ((t & 0x3Eu) << 2); }
__device__ __forceinline__ bool tag_ok_window(const u32x4& c, unsigned tlo) {
    const unsigned b3 = gather8(c[3]);
    const unsigned t = ((b3 >> 1) & 1u) | ((b3 >> 2) & 0x3Eu) | (gather8(c[0]) << 6) | (gather8(c[1]) << 14) | (gather8(c[2]) << 22);
    return ((b3 & 5u) == 4u) && (((t - tlo) & 0x3FFFFFFFu) <= 30u);
}

__global__ __launch_bounds__(512, 2) void sqllm_fused_kernel(const __hip_bfloat16* __restrict__ x,
                                                             const int* __restrict__ qw,
                                                             const __hip_bfloat16* __restrict__ lut,
                                                             __hip_bfloat16* __restrict__ out, char* ws) {
    __shared__ __attribute__((aligned(1024))) char smem[LDS_TOTAL];
    const int tid = threadIdx.x;
    const int wid = __builtin_amdgcn_readfirstlane(tid >> 6);
    const int lane = tid & 63;
    const int xcd = blockIdx.x & 7, idx = blockIdx.x >> 3;
    const int mtile = (xcd >> 1) * 4 + (idx & 3), ntile = (xcd & 1) * 8 + (idx >> 2);
    const int m0 = mtile * BM, n0 = ntile * BN;
    const unsigned lds_base = (unsigned)(uintptr_t)(__attribute__((address_space(3))) char*)smem;

    const int kh = wid & 1, ch = (wid >> 1) & 1, hh = wid >> 2;
    const int r = lane & 15, qd = lane >> 4;
    char* pk = ws + WS_PACK_OFF;
    char* ctr = ws + WS_CTR_OFF + ntile * 256;
    const int rr = lane < 56 ? lane / 28 : 1, cc = lane < 56 ? lane - rr * 28 : 27;

    unsigned ticket = 1;
    if (wid == 0) {
        if (lane == 0)
            asm volatile("global_atomic_add %0, %1, %0, %2 sc0 sc1" : "+v"(ticket) : "v"(0u), "s"(ctr) : "memory");
    }
    if (wid == 7) {
#pragma unroll
        for (int i = 0; i < 4; ++i)
            glds((unsigned)(rr * (N_ * 2) + cc * 16), (const char*)(lut + (size_t)2 * i * N_ + n0), lds_base + LDS_LUT + i * 1024);
    }
    const int t_ks = wid * 4 + (mtile >> 2), t_cb = (mtile >> 1) & 1, t_half = mtile & 1;
    const unsigned rawq = wid < NPRE ? LDS_RAWQ + wid * 8192 : LDS_RAWQ2 + (wid & 1) * 8192;
    auto issue_rawq = [&]() {
        const char* qb = (const char*)(qw + (size_t)(t_ks * 32 + t_half * 16) * N_ + n0 + t_cb * 112);
        const unsigned qo = (unsigned)(rr * (N_ * 4) + cc * 16);
#pragma unroll
        for (int i = 0; i < 8; ++i) glds_nt(qo, qb + (size_t)i * 2 * N_ * 4, lds_base + rawq + i * 1024);
    };
    if (wid < NPRE) issue_rawq();
    unsigned xo0, xo1;
    {
        const int f0 = (lane >> 4) & 7, f1 = (4 + (lane >> 4)) & 7;
        xo0 = (unsigned)((lane >> 3) * (K_ * 2) + (((lane & 7) ^ f0) << 4));
        xo1 = (unsigned)((lane >> 3) * (K_ * 2) + (((lane & 7) ^ f1) << 4) + 8 * K_ * 2);
    }
    const char* xb = (const char*)(x + (size_t)(m0 + wid * 16) * K_);
    const unsigned x_lds = lds_base + wid * 2048;
#pragma unroll
    for (int st = 0; st < 2; ++st) {
        issue_x1(xo0, xb + st * 128, x_lds + st * STAGE, 0, 1);
        issue_x1(xo1, xb + st * 128, x_lds + st * STAGE + 1024, 0, 1);
    }
    if (wid == 0) asm volatile("s_waitcnt vmcnt(12)" : "+v"(ticket) : : "memory");
    if (wid == 7) asm volatile("s_waitcnt vmcnt(%0)" : : "n"(NPRE > 7 ? 12 : 4) : "memory");
    if (wid == 0 && lane == 0) *(unsigned*)(smem + LDS_TICKET) = ticket;
    WAIT_LGKM0();
    BARRIER();
    STAMP(0, 2);
    ticket = __builtin_amdgcn_readfirstlane(*(const unsigned*)(smem + LDS_TICKET));
    u32x4 mytag;
    mytag[0] = spread8(ticket >> 6);
    mytag[1] = spread8(ticket >> 14);
    mytag[2] = spread8(ticket >> 22);
    mytag[3] = spread8(tag_b3(ticket));
    auto pack_publish = [&]() {
        const int ph = lane >> 5, q2 = (lane >> 4) & 1;
        const unsigned* rq = (const unsigned*)(smem + rawq) + q2 * 1024 + r;
        u32x4 o;
#pragma unroll
        for (int j = 0; j < 4; ++j) {
            const unsigned* p = rq + (j < 3 ? ph * 64 + 16 * j : 48);
            const unsigned s0 = p[0] | (p[112] << 8) | (p[256] << 16) | (p[368] << 24);
            const unsigned s1 = p[512] | (p[624] << 8) | (p[768] << 16) | (p[880] << 24);
            o[j] = s0 | (s1 << 4) | mytag[j];
        }
        const unsigned roff = (unsigned)((ph * 32 * 64 + (2 * t_half + q2) * 16 + r) * 16);
        const char* rb = pk + (size_t)(((ntile * 2 + t_cb) * 2 * 32 + t_ks) * 1024);
        asm volatile("global_store_dwordx4 %0, %1, %2 sc0 sc1\n\ts_nop 1" : : "v"(roff), "v"(o), "s"(rb) : "memory");
    };
    if (wid < NPRE) {
        WAIT_VM(4);
        STAMP(0, 3);
        pack_publish();
    }
    STAMP(0, 4);
#pragma unroll
    for (int st = 2; st < 4; ++st) {
        issue_x1(xo0, xb + st * 128, x_lds + st * STAGE, 0, 1);
        issue_x1(xo1, xb + st * 128, x_lds + st * STAGE + 1024, 0, 1);
    }
    u32x4 L[4];
#pragma unroll
    for (int j = 0; j < 4; ++j) {
        const int col = ch * 112 + (j < 3 ? hh * 64 + 16 * j : 48) + r;
        const unsigned short* lp = (const unsigned short*)(smem + LDS_LUT) + col;
        unsigned e[4];
#pragma unroll
        for (int c = 0; c < 4; ++c) e[c] = (unsigned)lp[c * 512] | ((unsigned)lp[c * 512 + 224] << 16);
        L[j][0] = perm(e[1], e[0], 0x06040200u);
        L[j][1] = perm(e[3], e[2], 0x06040200u);
        L[j][2] = perm(e[1], e[0], 0x07050301u);
        L[j][3] = perm(e[3], e[2], 0x07050301u);
    }

    const unsigned coff = (unsigned)(lane * 16);
    const char* pb = pk + (size_t)((((ntile * 2 + ch) * 2 + hh) * 32 + kh) * 1024);
    constexpr size_t CSTEP = 2048;
    u32x4 c0 = {}, c1 = {};
#pragma unroll 1
    for (int spin = 0; spin < (1 << 16); ++spin) {
        asm volatile(
            "global_load_dwordx4 %0, %2, %3 sc1\n\t"
            "global_load_dwordx4 %1, %2, %3 offset:2048 sc1\n\t"
            "s_waitcnt vmcnt(0)"
            : "+{" C0REG "}"(c0), "+{" C1REG "}"(c1)
            : "v"(coff), "s"(pb)
            : "memory");
        const bool ok = tag_ok_window(c0, ticket - 15u) && tag_ok_window(c1, ticket - 15u);
        if (__builtin_amdgcn_ballot_w64(ok) == ~0ull) break;
        __builtin_amdgcn_s_sleep(1);
    }
    STAMP(0, 5);
    const unsigned e0 = c0[3], e1 = c1[3];

    f32x4 acc[8][4];
#pragma unroll
    for (int i = 0; i < 8; ++i)
#pragma unroll
        for (int j = 0; j < 4; ++j)
            if (i < 4 || j < 3) {
                acc[i][j] = (f32x4){0.f, 0.f, 0.f, 0.f};
                asm volatile("" : "+v"(acc[i][j]));
            }

    const unsigned xa = (unsigned)(r * 128 + (((4 * kh + qd) ^ ((r >> 1) & 7)) << 4));
    const unsigned rbx = (unsigned)((4 * hh + 2 * kh) * 2048);
    unsigned xb4[4];
#pragma unroll
    for (int i2 = 0; i2 < 4; ++i2) xb4[i2] = xa + ((unsigned)(i2 << 12) ^ rbx);

    BARRIER();
    bf16x8 wA[4], wB[4], xf[8];
#pragma unroll
    for (int i = 0; i < 6; ++i) xf[i] = *(const bf16x8*)(smem + xb4[i >> 1] + (i & 1) * 2048);
#pragma unroll
    for (int j = 0; j < 4; ++j) wA[j] = lookup(c0[j] & 0x07070707u, (c0[j] >> 4) & 0x07070707u, L[j]);
    SCHED0();
    LOAD_CODES(C0REG, c0, coff, pb + 2 * CSTEP);
    STAMP(0, 6);

    auto mrow = [&](int i, const bf16x8(&w)[4]) {
#pragma unroll
        for (int j = 0; j < 4; ++j)
            if (i < 4 || j < 3) acc[i][j] = __builtin_amdgcn_mfma_f32_16x16x32_bf16(w[j], xf[i], acc[i][j], 0, 0, 0);
    };
    auto pattern = [&](int i) {
        __builtin_amdgcn_sched_group_barrier(0x008, 1, 0);
        __builtin_amdgcn_sched_group_barrier(0x100, 1, 0);
        __builtin_amdgcn_sched_group_barrier(0x002, 1, 0);
        __builtin_amdgcn_sched_group_barrier(0x008, 1, 0);
        __builtin_amdgcn_sched_group_barrier(0x100, 1, 0);
        __builtin_amdgcn_sched_group_barrier(0x002, 1, 0);
#pragma unroll
        for (int j = 0; j < (i < 4 ? 6 : 4); ++j) {
            __builtin_amdgcn_sched_group_barrier(0x008, 1, 0);
            __builtin_amdgcn_sched_group_barrier(0x002, 2, 0);
        }
        SCHED0();
    };
    auto rdmm = [&](int i, const bf16x8(&w)[4], unsigned ra) {
        xf[(i + 6) & 7] = *(const bf16x8*)(smem + ra);
        xf[(i + 7) & 7] = *(const bf16x8*)(smem + ra + 2048);
        mrow(i, w);
        mrow(i + 1, w);
    };
#define PRIO(p) __builtin_amdgcn_s_setprio(p)
#define PIN(x) asm volatile("" : "+v"(x))
#define UNPACK(c)                                              \
    _Pragma("unroll") for (int j = 0; j < 4; ++j) {            \
        sl[j] = (c)[j] & 0x07070707u;                          \
        sh[j] = ((c)[j] >> 4) & 0x07070707u;                   \
    }

    unsigned a0 = xb4[0], a1 = xb4[1], a2 = xb4[2], a3 = xb4[3];
    unsigned sl[4], sh[4];
    PRIO(3);
#pragma unroll 1
    for (int u = 0; u < NST; ++u) {
        const char* xn = xb + (u + 2) * 256;
        const unsigned xl = x_lds + ((u + 2) & 3) * ST_BYTES;
        const unsigned nx = (unsigned)((u + 1) & 3) * ST_BYTES;
        WAIT_VERIFY(C1REG, C1D3, c1, 5, 1, 5, u, 6, u, u, e1, coff, pb + (size_t)(2 * u + 1) * CSTEP);
        if (NPRE < 8) {
            if (wid == u + NPRE - 1 && u > 0) {
                pack_publish();
            }
            if (wid == u + NPRE) issue_rawq();
        }
        SCHED0();
        rdmm(0, wA, a3);
        UNPACK(c1);
        pattern(0);
        LOAD_CODES(C1REG, c1, coff, pb + (size_t)((2 * u + 3) & 15) * CSTEP);
        PRIO(2);
        rdmm(2, wA, a0 + STAGE);
        wB[0] = lookup(sl[0], sh[0], L[0]);
        wB[1] = lookup(sl[1], sh[1], L[1]);
        pattern(2);
        PIN(wB[0]); PIN(wB[1]);
        issue_x1(xo0, xn, xl, u, 5);
        a0 = xb4[0] + nx;
        rdmm(4, wA, a1 + STAGE);
        wB[2] = lookup(sl[2], sh[2], L[2]);
        pattern(4);
        PIN(wB[2]);
        issue_x1(xo1, xn, xl + 1024, u, 5);
        PRIO(1);
        a1 = xb4[1] + nx;
        rdmm(6, wA, a2 + STAGE);
        wB[3] = lookup(sl[3], sh[3], L[3]);
        pattern(6);
        PIN(wB[3]);
        WAIT_VERIFY(C0REG, C0D3, c0, 3, 1, 11, u, 5, wid - NPRE, u, e0, coff, pb + (size_t)((2 * u + 2) & 15) * CSTEP);
        PRIO(0);
        SCHED0();
        a2 = xb4[2] + nx;
        rdmm(0, wB, a3 + STAGE);
        UNPACK(c0);
        pattern(0);
        LOAD_CODES(C0REG, c0, coff, pb + (size_t)((2 * u + 4) & 15) * CSTEP);
        BARRIER();
        PRIO(3);
        STAMPF(0, 8 + u);
        SCHED0();
        a3 = xb4[3] + nx;
        rdmm(2, wB, a0);
        wA[0] = lookup(sl[0], sh[0], L[0]);
        wA[1] = lookup(sl[1], sh[1], L[1]);
        pattern(2);
        PIN(wA[0]); PIN(wA[1]);
        issue_x1(xo0, xn + 128, xl + STAGE, u, 5);
        rdmm(4, wB, a1);
        wA[2] = lookup(sl[2], sh[2], L[2]);
        pattern(4);
        PIN(wA[2]);
        issue_x1(xo1, xn + 128, xl + STAGE + 1024, u, 5);
        rdmm(6, wB, a2);
        wA[3] = lookup(sl[3], sh[3], L[3]);
        pattern(6);
        PIN(wA[3]);
    }
    PRIO(0);
    STAMP(0, 20);

    asm volatile("s_waitcnt vmcnt(0) lgkmcnt(0)" : "+{" C0REG "}"(c0), "+{" C1REG "}"(c1) :: "memory");
    BARRIER();
    if (tid == 0) {
        asm volatile("global_atomic_add %0, %1, %2 sc1" : : "v"(0u), "v"(1u), "s"(ctr) : "memory");
    }
    {
        char* xs = smem + wid * 14336 + lane * 16;
        int e = 0;
#pragma unroll
        for (int i = 0; i < 8; ++i)
            if (i & 2) {
#pragma unroll
                for (int j = 0; j < 4; ++j)
                    if (i < 4 || j < 3) { *(f32x4*)(xs + e * 1024) = acc[i][j]; ++e; }
            }
    }
    WAIT_LGKM0();
    BARRIER();
    STAMP(0, 21);
    {
        const char* xr = smem + (wid ^ 1) * 14336 + lane * 16;
        int e = 0;
#pragma unroll
        for (int i = 0; i < 8; ++i)
            if (!(i & 2)) {
#pragma unroll
                for (int j = 0; j < 4; ++j)
                    if (i < 4 || j < 3) { acc[i][j] += *(const f32x4*)(xr + e * 1024); ++e; }
            }
    }
    WAIT_LGKM0();
    BARRIER();
    {
#pragma unroll
        for (int i = 0; i < 8; ++i)
            if (!(i & 2)) {
                const int rb = i ^ (4 * hh) ^ (2 * kh);
#pragma unroll
                for (int j = 0; j < 4; ++j)
                    if (i < 4 || j < 3) {
                        bf16x4 v;
                        v[0] = (__bf16)acc[i][j][0];
                        v[1] = (__bf16)acc[i][j][1];
                        v[2] = (__bf16)acc[i][j][2];
                        v[3] = (__bf16)acc[i][j][3];
                        const int col = ch * 112 + (j < 3 ? hh * 64 + 16 * j : 48) + qd * 4;
                        *(bf16x4*)(smem + (rb * 16 + r) * OUT_STRIDE + col * 2) = v;
                    }
            }
    }
    WAIT_LGKM0();
    BARRIER();
    STAMP(0, 22);
    {
        const int row = wid * 16 + rr;
        const char* src = smem + row * OUT_STRIDE + cc * 16;
        const unsigned doff = (unsigned)(rr * (N_ * 2) + cc * 16);
        const char* dst = (const char*)(out + (size_t)(m0 + wid * 16) * N_ + n0);
        if (lane < 56) {
#pragma unroll
            for (int it = 0; it < 8; ++it) {
                const u32x4 v = *(const u32x4*)(src + it * 2 * OUT_STRIDE);
                asm volatile("global_store_dwordx4 %0, %1, %2" STPOL "\n\ts_nop 1" : : "v"(doff), "v"(v), "s"(dst + (size_t)it * 2 * N_ * 2) : "memory");
            }
        }
    }
}

}

extern "C" void kernel_launch(void* const* d_in, const int* in_sizes, int n_in, void* d_out, int out_size,
                              void* d_ws, size_t ws_size, hipStream_t stream) {
    const __hip_bfloat16* x = (const __hip_bfloat16*)d_in[0];
    const int* qw = (const int*)d_in[1];
    const __hip_bfloat16* lut = (const __hip_bfloat16*)d_in[2];
    __hip_bfloat16* out = (__hip_bfloat16*)d_out;
    sqllm_fused_kernel<<<dim3(256), dim3(512), 0, stream>>>(x, qw, lut, out, (char*)d_ws);
}
```

```cpp
#include <hip/hip_runtime.h>
#include <hip/hip_bf16.h>
#include <stdint.h>

typedef __attribute__((ext_vector_type(4))) __bf16 bf16x4;
typedef __attribute__((ext_vector_type(4))) float f32x4;
typedef __attribute__((ext_vector_type(32))) float f32x32;
typedef __attribute__((ext_vector_type(4))) unsigned u32x4;
typedef __attribute__((ext_vector_type(8))) unsigned u32x8;
typedef __attribute__((ext_vector_type(16))) unsigned u32x16;

namespace {

constexpr int M_ = 2048, K_ = 1024, N_ = 3584;
constexpr int BM = 128, BN = 224;
constexpr int STAGE = 128 * 128;
constexpr int LDS_RING = 8 * STAGE;
constexpr int LDS_LUT = LDS_RING;
constexpr int LDS_RAWQ = LDS_RING + 4096;
constexpr int LDS_TICKET = LDS_RAWQ + 3 * 8192;
constexpr int LDS_TOTAL = LDS_RING + 2 * 14848;
static_assert(LDS_TICKET + 64 <= LDS_TOTAL, "");
constexpr int OUT_STRIDE = 464;
constexpr size_t WS_PACK_OFF = 1 << 20;
constexpr size_t WS_CTR_OFF = 4 << 20;
constexpr unsigned TAGM = 0x88888888u;

#define STAMP(slot) do {} while (0)
#define WAIT_LGKM0() asm volatile("s_waitcnt lgkmcnt(0)" ::: "memory")
#define WAIT_VM(n) asm volatile("s_waitcnt vmcnt(" #n ")" ::: "memory")
#define BARRIER()                          \
    do {                                   \
        asm volatile("" ::: "memory");     \
        __builtin_amdgcn_s_barrier();      \
        asm volatile("" ::: "memory");     \
    } while (0)

__device__ __forceinline__ unsigned perm(unsigned hi, unsigned lo, unsigned sel) { return __builtin_amdgcn_perm(hi, lo, sel); }

#define GLDS(POL, voff, sbase, lds_dst)                                                               \
    asm volatile("s_mov_b32 m0, %2\n\ts_nop 0\n\tglobal_load_lds_dwordx4 %0, %1" POL                  \
                 :                                                                                    \
                 : "v"(voff), "s"(sbase), "s"(lds_dst)                                                \
                 : "memory")

__device__ __forceinline__ unsigned spread8(unsigned b) {
    unsigned x = b & 0xffu;
    x = (x | (x << 12)) & 0x000F000Fu;
    x = (x | (x << 6)) & 0x03030303u;
    x = (x | (x << 3)) & 0x11111111u;
    return x << 3;
}
__device__ __forceinline__ unsigned expand24(unsigned t) {
    unsigned x = t & 0xFFFFFFu;
    x = (x & 0xFFFu) | ((x & 0xFFF000u) << 4);
    x = (x & 0x003F003Fu) | ((x & 0x0FC00FC0u) << 2);
    x = (x & 0x07070707u) | ((x & 0x38383838u) << 1);
    return x << 1;
}
__device__ __forceinline__ unsigned tag_b3(unsigned t) { return ((t & 1u) << 1) | 4u | ((t & 0x3Eu) << 2); }
__device__ __forceinline__ bool tag_ok_window(unsigned c0, unsigned c1, unsigned c2, unsigned c3, unsigned elo, unsigned espan) {
    unsigned y = (c1 & TAGM) | ((c0 >> 1) & ~TAGM);
    y = (c2 & TAGM) | ((y >> 1) & ~TAGM);
    return ((y & 0xEEEEEEEEu) - elo <= espan) && ((c3 & 0x808u) == 0x800u);
}
__device__ __forceinline__ u32x4 make_srd(const void* base, unsigned bytes) {
    const unsigned long long a = (unsigned long long)base;
    u32x4 r;
    r[0] = __builtin_amdgcn_readfirstlane((unsigned)a);
    r[1] = __builtin_amdgcn_readfirstlane((unsigned)(a >> 32) & 0xFFFFu);
    r[2] = __builtin_amdgcn_readfirstlane(bytes);
    r[3] = 0x00020000u;
    return r;
}

struct Acc {
    f32x32 a[4];
};

__global__ __launch_bounds__(512, 2) void sqllm_fused_kernel(const __hip_bfloat16* __restrict__ x, const int* __restrict__ qw,
                                                             const __hip_bfloat16* __restrict__ lut, __hip_bfloat16* __restrict__ out,
                                                             char* ws) {
    __shared__ __attribute__((aligned(1024))) char smem[LDS_TOTAL];
    __builtin_amdgcn_s_setprio(3);
    const int tid = threadIdx.x;
    const int wid = __builtin_amdgcn_readfirstlane(tid >> 6);
    const int lane = tid & 63;
    const int xcd = blockIdx.x & 7, idx = blockIdx.x >> 3;
    const int mtile = (xcd >> 1) * 4 + (idx & 3), ntile = (xcd & 1) * 8 + (idx >> 2);
    const int m0 = mtile * BM, n0 = ntile * BN;

    const int kh = wid & 1, ch = (wid >> 1) & 1, hh = wid >> 2;
    const int r = lane & 15, qd = lane >> 4;
    char* pk = ws + WS_PACK_OFF;
    char* ctr = ws + WS_CTR_OFF + ntile * 256;
    const int rr = lane < 56 ? lane / 28 : 1, cc = lane < 56 ? lane - rr * 28 : 27;

    const int t_ksl = mtile >> 2, t_cb = (mtile >> 1) & 1, t_half = mtile & 1;
    const unsigned qb_off = (unsigned)(((t_ksl * 32 + t_half * 16 + wid * 2) * N_ + n0 + t_cb * 112) * 4);
    const char* qb = (const char*)qw + qb_off;
    const unsigned qo = (unsigned)(rr * (N_ * 4) + cc * 16);
    constexpr size_t QST = (size_t)128 * N_ * 4;
    const unsigned q_lds = LDS_RAWQ + wid * 1024;
    GLDS(" nt", qo, qb + 0 * QST, q_lds + 0 * 8192);
    GLDS(" nt", qo, qb + 1 * QST, q_lds + 1 * 8192);
    unsigned ticket = 1;
    if (wid == 0) {
        if (lane == 0) asm volatile("global_atomic_add %0, %1, %0, %2 sc0 sc1" : "+v"(ticket) : "v"(0u), "s"(ctr) : "memory");
    }
    if (wid >= 4) GLDS("", (unsigned)(rr * (N_ * 2) + cc * 16), (const char*)(lut + (size_t)2 * (wid - 4) * N_ + n0), (unsigned)(LDS_LUT + (wid - 4) * 1024));
    const int f0 = (lane >> 4) & 7, f1 = (4 + (lane >> 4)) & 7;
    const unsigned xo0 = (unsigned)((lane >> 3) * (K_ * 2) + (((lane & 7) ^ f0) << 4));
    const unsigned xo1 = (unsigned)((lane >> 3) * (K_ * 2) + (((lane & 7) ^ f1) << 4) + 8 * K_ * 2);
    const unsigned xb_off = (unsigned)((m0 + wid * 16) * K_ * 2);
    const char* xb = (const char*)x + xb_off;
    const unsigned x_lds = wid * 2048;
#pragma unroll
    for (int st = 0; st < 2; ++st) {
        GLDS("", xo0, xb + st * 128, x_lds + st * STAGE);
        GLDS("", xo1, xb + st * 128, x_lds + st * STAGE + 1024);
    }
    GLDS(" nt", qo, qb + 2 * QST, q_lds + 2 * 8192);

    const int pj = lane & 3, prl = (lane >> 2) & 7, psh = lane >> 5;
    const unsigned p_rd = (unsigned)(LDS_RAWQ + ((ch * 4 + psh * 2) * 256 + (pj < 3 ? hh * 64 + 16 * pj : 48) + kh * 8 + prl) * 4);
    const unsigned p_st = (unsigned)(((2 * t_half + ch) * 16 + kh * 8 + prl) * 16 + pj * 4);
    const char* p_reg = pk + (size_t)((ntile * 2 + t_cb) * 2 + hh) * 32768;

    Acc A;
#pragma unroll
    for (int i = 0; i < 4; ++i)
#pragma unroll
        for (int e = 0; e < 32; ++e) A.a[i][e] = 0.f;
    asm volatile("" : "+{v[0:31]}"(A.a[0]), "+{v[32:63]}"(A.a[1]), "+{v[64:95]}"(A.a[2]), "+{v[96:127]}"(A.a[3]));
    asm volatile("s_waitcnt vmcnt(5)" : "+v"(ticket) : : "memory");
    if (wid == 0 && lane == 0) *(unsigned*)(smem + LDS_TICKET) = ticket;
    WAIT_LGKM0();
    BARRIER();
    STAMP(2);
    ticket = __builtin_amdgcn_readfirstlane(*(const unsigned*)(smem + LDS_TICKET));
    const unsigned mytag = pj == 3 ? spread8(tag_b3(ticket)) : ((expand24(ticket) << (2 - pj)) & TAGM);
    const unsigned elo = expand24(ticket - 15u), espan = expand24(ticket + 15u) - elo;
    auto pack = [&](int s) {
        const unsigned* p = (const unsigned*)(smem + p_rd + s * 8192);
        const unsigned v = p[0] | (p[112] << 8) | (p[256] << 16) | (p[368] << 24);
        const auto sw = __builtin_amdgcn_permlane32_swap(v, v, false, false);
        const unsigned o = sw[0] | (sw[1] << 4) | mytag;
        asm volatile("global_store_dword %0, %1, %2 sc0 sc1" : : "v"(p_st), "v"(o), "s"(p_reg + (size_t)(4 * s + t_ksl) * 1024) : "memory");
    };
    pack(0);
    pack(1);
    STAMP(3);
    u32x16 L;
#pragma unroll
    for (int j = 0; j < 4; ++j) {
        const int col = ch * 112 + (j < 3 ? hh * 64 + 16 * j : 48) + r;
        const unsigned short* lp = (const unsigned short*)(smem + LDS_LUT) + col;
        unsigned e[4];
#pragma unroll
        for (int c = 0; c < 4; ++c) e[c] = (unsigned)lp[c * 512] | ((unsigned)lp[c * 512 + 224] << 16);
        L[4 * j + 0] = perm(e[1], e[0], 0x06040200u);
        L[4 * j + 1] = perm(e[3], e[2], 0x06040200u);
        L[4 * j + 2] = perm(e[1], e[0], 0x07050301u);
        L[4 * j + 3] = perm(e[3], e[2], 0x07050301u);
    }
    asm volatile("" : "+{v[192:207]}"(L));
    STAMP(4);
    const unsigned coff = (unsigned)(lane * 16);
    const char* pb = pk + (size_t)((((ntile * 2 + ch) * 2 + hh) * 32 + kh) * 1024);
    u32x8 C = {};
#pragma unroll 1
    for (int spin = 0; spin < (1 << 16); ++spin) {
        asm volatile(
            "global_load_dwordx4 v[208:211], %1, %2 sc1\n\t"
            "global_load_dwordx4 v[212:215], %1, %2 offset:2048 sc1\n\t"
            "s_waitcnt vmcnt(0)"
            : "+{v[208:215]}"(C)
            : "v"(coff), "s"(pb)
            : "memory");
        const bool ok = (int)tag_ok_window(C[0], C[1], C[2], C[3], elo, espan) & (int)tag_ok_window(C[4], C[5], C[6], C[7], elo, espan);
        if (__builtin_amdgcn_ballot_w64(ok) == ~0ull) break;
        __builtin_amdgcn_s_sleep(1);
    }
    STAMP(5);
#pragma unroll
    for (int st = 2; st < 4; ++st) {
        GLDS("", xo0, xb + st * 128, x_lds + st * STAGE);
        GLDS("", xo1, xb + st * 128, x_lds + st * STAGE + 1024);
    }
    BARRIER();
    GLDS(" nt", qo, qb + 3 * QST, q_lds + 0 * 8192);
    pack(2);
    STAMP(6);

    const unsigned xa = (unsigned)(r * 128 + (((4 * kh + qd) ^ ((r >> 1) & 7)) << 4));
    u32x16 MI = {};
    MI[0] = xa + 4096u * kh;
    MI[1] = xa + 4096u * (1 - kh);
    MI[2] = xo0;
    MI[3] = xo1;
    MI[4] = qo;
    MI[5] = p_rd;
    MI[6] = p_st;
    MI[7] = mytag;
    u32x8 MJ = {};
    MJ[0] = coff;
    MJ[1] = C[3];
    MJ[2] = C[7];
    MJ[3] = (unsigned)((kh * 16 + r) * OUT_STRIDE + (ch * 112 + hh * 64 + qd * 4) * 2) |
            ((unsigned)((wid * 4 + rr) * OUT_STRIDE + cc * 16) << 16);
    MJ[4] = (unsigned)(rr * (N_ * 2) + cc * 16);
    const unsigned ob_off = (unsigned)(((m0 + ((wid * 4) & 15)) * N_ + n0) * 2);
    const u32x4 xr = make_srd(xb, (unsigned)(M_ * K_ * 2) - xb_off);
    const u32x4 qr = make_srd(qb, (unsigned)(K_ * N_ * 4) - qb_off);
    const u32x4 rrd = make_srd(pb, 32768u - 1024u * kh);
    const u32x4 sr = make_srd(p_reg, 32768u);
    const u32x4 orr = make_srd((const char*)out + ob_off, (unsigned)(M_ * N_ * 2) - ob_off);
    asm volatile(
    "s_cmp_lg_u32 %[hh], 0\n\t"
    "s_cbranch_scc1 .Lrole3_%=\n\t"
    "s_mov_b32 s58, %[xk]\n\t"
    "s_mov_b32 s61, %[ql]\n\t"
    "s_mov_b32 s62, %[so]\n\t"
    "s_movk_i32 s56, 0x200\n\t"
    "s_mov_b32 s60, 0x700000\n\t"
    "s_movk_i32 s59, 0x1000\n\t"
    "s_mov_b32 s63, 0\n\t"
    "s_mov_b32 s64, 0x88888888\n\t"
    "s_mov_b32 s65, 0x05010400\n\t"
    "s_mov_b32 s66, 0x07030602\n\t"
    "s_mov_b32 s67, 0x07070707\n\t"
    "s_mov_b32 s68, 0\n\t"
    "v_mov_b32 v253, v232\n\t"
    "v_mov_b32 v254, v233\n\t"
    "ds_read_b128 v[128:131], v253 offset:0\n\t"
    "ds_read_b128 v[132:135], v253 offset:2048\n\t"
    "ds_read_b128 v[136:139], v254 offset:0\n\t"
    "ds_read_b128 v[140:143], v254 offset:2048\n\t"
    "ds_read_b128 v[144:147], v253 offset:8192\n\t"
    "ds_read_b128 v[148:151], v253 offset:10240\n\t"
    "ds_read_b128 v[152:155], v254 offset:8192\n\t"
    "v_and_b32 v216, s67, v208\n\t"
    "v_lshrrev_b32 v217, 4, v208\n\t"
    "v_and_b32 v218, s67, v209\n\t"
    "v_lshrrev_b32 v219, 4, v209\n\t"
    "v_and_b32 v220, s67, v210\n\t"
    "v_lshrrev_b32 v221, 4, v210\n\t"
    "v_and_b32 v222, s67, v211\n\t"
    "v_lshrrev_b32 v223, 4, v211\n\t"
    "buffer_load_dwordx4 v[208:211], v248, s[48:51], s59 offen sc1\n\t"
    "v_and_b32 v217, s67, v217\n\t"
    "v_and_b32 v219, s67, v219\n\t"
    "v_and_b32 v221, s67, v221\n\t"
    "v_and_b32 v223, s67, v223\n\t"
    "v_perm_b32 v224, v193, v192, v216\n\t"
    "v_perm_b32 v225, v195, v194, v216\n\t"
    "v_perm_b32 v226, v193, v192, v217\n\t"
    "v_perm_b32 v227, v195, v194, v217\n\t"
    "v_perm_b32 v160, v225, v224, s65\n\t"
    "v_perm_b32 v161, v225, v224, s66\n\t"
    "v_perm_b32 v162, v227, v226, s65\n\t"
    "v_perm_b32 v163, v227, v226, s66\n\t"
    "v_perm_b32 v228, v197, v196, v218\n\t"
    "v_perm_b32 v229, v199, v198, v218\n\t"
    "v_perm_b32 v230, v197, v196, v219\n\t"
    "v_perm_b32 v231, v199, v198, v219\n\t"
    "v_perm_b32 v164, v229, v228, s65\n\t"
    "v_perm_b32 v165, v229, v228, s66\n\t"
    "v_perm_b32 v166, v231, v230, s65\n\t"
    "v_perm_b32 v167, v231, v230, s66\n\t"
    "v_perm_b32 v224, v201, v200, v220\n\t"
    "v_perm_b32 v225, v203, v202, v220\n\t"
    "v_perm_b32 v226, v201, v200, v221\n\t"
    "v_perm_b32 v227, v203, v202, v221\n\t"
    "v_perm_b32 v168, v225, v224, s65\n\t"
    "v_perm_b32 v169, v225, v224, s66\n\t"
    "v_perm_b32 v170, v227, v226, s65\n\t"
    "v_perm_b32 v171, v227, v226, s66\n\t"
    "v_perm_b32 v228, v205, v204, v222\n\t"
    "v_perm_b32 v229, v207, v206, v222\n\t"
    "v_perm_b32 v230, v205, v204, v223\n\t"
    "v_perm_b32 v231, v207, v206, v223\n\t"
    "v_perm_b32 v172, v229, v228, s65\n\t"
    "v_perm_b32 v173, v229, v228, s66\n\t"
    "v_perm_b32 v174, v231, v230, s65\n\t"
    "v_perm_b32 v175, v231, v230, s66\n\t"
    "s_setprio 2\n\t"
    "s_nop 1\n\t"
    ".Lloop_4_%=:\n\t"
    "s_waitcnt lgkmcnt(3)\n\t"
    "v_mfma_f32_16x16x32_bf16 v[0:3], v[160:163], v[128:131], v[0:3]\n\t"
    "ds_read_b128 v[156:159], v254 offset:10240\n\t"
    "v_mfma_f32_16x16x32_bf16 v[4:7], v[164:167], v[128:131], v[4:7]\n\t"
    "s_waitcnt vmcnt(7)\n\t"
    "v_bitop3_b32 v255, v215, v250, s64 bitop3:0x28\n\t"
    "v_cmp_ne_u32 vcc, 0, v255\n\t"
    "v_mfma_f32_16x16x32_bf16 v[8:11], v[168:171], v[128:131], v[8:11]\n\t"
    "s_cbranch_vccnz .Lslow0_4_%=\n\t"
    ".Lback0_4_%=:\n\t"
    "v_and_b32 v216, s67, v212\n\t"
    "v_lshrrev_b32 v217, 4, v212\n\t"
    "v_mfma_f32_16x16x32_bf16 v[12:15], v[172:175], v[128:131], v[12:15]\n\t"
    "v_and_b32 v218, s67, v213\n\t"
    "v_lshrrev_b32 v219, 4, v213\n\t"
    "v_mfma_f32_16x16x32_bf16 v[16:19], v[160:163], v[132:135], v[16:19]\n\t"
    "v_and_b32 v220, s67, v214\n\t"
    "v_lshrrev_b32 v221, 4, v214\n\t"
    "ds_read_b128 v[128:131], v253 offset:16384\n\t"
    "v_mfma_f32_16x16x32_bf16 v[20:23], v[164:167], v[132:135], v[20:23]\n\t"
    "v_and_b32 v222, s67, v215\n\t"
    "v_lshrrev_b32 v223, 4, v215\n\t"
    "s_xor_b32 s57, s63, s58\n\t"
    "buffer_load_dwordx4 v[212:215], v248, s[48:51], s59 offen offset:2048 sc1\n\t"
    "v_mfma_f32_16x16x32_bf16 v[24:27], v[168:171], v[132:135], v[24:27]\n\t"
    "v_and_b32 v217, s67, v217\n\t"
    "v_and_b32 v219, s67, v219\n\t"
    "v_mfma_f32_16x16x32_bf16 v[28:31], v[172:175], v[132:135], v[28:31]\n\t"
    "v_and_b32 v221, s67, v221\n\t"
    "v_and_b32 v223, s67, v223\n\t"
    "v_mfma_f32_16x16x32_bf16 v[32:35], v[160:163], v[136:139], v[32:35]\n\t"
    "v_perm_b32 v224, v193, v192, v216\n\t"
    "v_perm_b32 v225, v195, v194, v216\n\t"
    "ds_read_b128 v[132:135], v253 offset:18432\n\t"
    "v_mfma_f32_16x16x32_bf16 v[36:39], v[164:167], v[136:139], v[36:39]\n\t"
    "v_perm_b32 v226, v193, v192, v217\n\t"
    "s_add_u32 s63, s63, 0x8000\n\t"
    "s_and_b32 s63, s63, 0x18000\n\t"
    "v_mfma_f32_16x16x32_bf16 v[40:43], v[168:171], v[136:139], v[40:43]\n\t"
    "v_perm_b32 v227, v195, v194, v217\n\t"
    "v_perm_b32 v176, v225, v224, s65\n\t"
    "s_setprio 2\n\t"
    "v_mfma_f32_16x16x32_bf16 v[44:47], v[172:175], v[136:139], v[44:47]\n\t"
    "v_perm_b32 v177, v225, v224, s66\n\t"
    "v_perm_b32 v178, v227, v226, s65\n\t"
    "v_mfma_f32_16x16x32_bf16 v[48:51], v[160:163], v[140:143], v[48:51]\n\t"
    "v_perm_b32 v179, v227, v226, s66\n\t"
    "v_perm_b32 v228, v197, v196, v218\n\t"
    "ds_read_b128 v[136:139], v254 offset:16384\n\t"
    "v_mfma_f32_16x16x32_bf16 v[52:55], v[164:167], v[140:143], v[52:55]\n\t"
    "v_perm_b32 v229, v199, v198, v218\n\t"
    "v_perm_b32 v230, v197, v196, v219\n\t"
    "v_mfma_f32_16x16x32_bf16 v[56:59], v[168:171], v[140:143], v[56:59]\n\t"
    "v_perm_b32 v231, v199, v198, v219\n\t"
    "v_perm_b32 v180, v229, v228, s65\n\t"
    "v_mfma_f32_16x16x32_bf16 v[60:63], v[172:175], v[140:143], v[60:63]\n\t"
    "v_perm_b32 v181, v229, v228, s66\n\t"
    "v_perm_b32 v182, v231, v230, s65\n\t"
    "s_waitcnt lgkmcnt(3)\n\t"
    "v_mfma_f32_16x16x32_bf16 v[64:67], v[160:163], v[144:147], v[64:67]\n\t"
    "v_perm_b32 v183, v231, v230, s66\n\t"
    "v_perm_b32 v224, v201, v200, v220\n\t"
    "ds_read_b128 v[140:143], v254 offset:18432\n\t"
    "v_mfma_f32_16x16x32_bf16 v[68:71], v[164:167], v[144:147], v[68:71]\n\t"
    "v_perm_b32 v225, v203, v202, v220\n\t"
    "v_perm_b32 v226, v201, v200, v221\n\t"
    "s_add_u32 m0, s57, 0\n\t"
    "v_mfma_f32_16x16x32_bf16 v[72:75], v[168:171], v[144:147], v[72:75]\n\t"
    "v_perm_b32 v227, v203, v202, v221\n\t"
    "v_perm_b32 v184, v225, v224, s65\n\t"
    "buffer_load_dwordx4 v234, s[40:43], s56 offen lds\n\t"
    "v_mfma_f32_16x16x32_bf16 v[76:79], v[172:175], v[144:147], v[76:79]\n\t"
    "v_perm_b32 v185, v225, v224, s66\n\t"
    "v_perm_b32 v186, v227, v226, s65\n\t"
    "v_mfma_f32_16x16x32_bf16 v[80:83], v[160:163], v[148:151], v[80:83]\n\t"
    "v_perm_b32 v187, v227, v226, s66\n\t"
    "v_perm_b32 v228, v205, v204, v222\n\t"
    "ds_read_b128 v[144:147], v253 offset:24576\n\t"
    "v_mfma_f32_16x16x32_bf16 v[84:87], v[164:167], v[148:151], v[84:87]\n\t"
    "v_perm_b32 v229, v207, v206, v222\n\t"
    "v_perm_b32 v230, v205, v204, v223\n\t"
    "s_add_u32 s59, s59, 0x1000\n\t"
    "v_mfma_f32_16x16x32_bf16 v[88:91], v[168:171], v[148:151], v[88:91]\n\t"
    "v_perm_b32 v231, v207, v206, v223\n\t"
    "v_perm_b32 v188, v229, v228, s65\n\t"
    "s_setprio 1\n\t"
    "v_mfma_f32_16x16x32_bf16 v[92:95], v[172:175], v[148:151], v[92:95]\n\t"
    "v_perm_b32 v189, v229, v228, s66\n\t"
    "v_perm_b32 v190, v231, v230, s65\n\t"
    "v_mfma_f32_16x16x32_bf16 v[96:99], v[160:163], v[152:155], v[96:99]\n\t"
    "v_perm_b32 v191, v231, v230, s66\n\t"
    "ds_read_b128 v[148:151], v253 offset:26624\n\t"
    "v_mfma_f32_16x16x32_bf16 v[100:103], v[164:167], v[152:155], v[100:103]\n\t"
    "s_add_u32 m0, s57, 1024\n\t"
    "v_mfma_f32_16x16x32_bf16 v[104:107], v[168:171], v[152:155], v[104:107]\n\t"
    "buffer_load_dwordx4 v235, s[40:43], s56 offen lds\n\t"
    "v_mfma_f32_16x16x32_bf16 v[108:111], v[172:175], v[152:155], v[108:111]\n\t"
    "v_mfma_f32_16x16x32_bf16 v[112:115], v[160:163], v[156:159], v[112:115]\n\t"
    "ds_read_b128 v[152:155], v254 offset:24576\n\t"
    "s_setprio 0\n\t"
    "v_mfma_f32_16x16x32_bf16 v[116:119], v[164:167], v[156:159], v[116:119]\n\t"
    "v_add_u32 v253, s63, v232\n\t"
    "v_add_u32 v244, s68, v237\n\t"
    "v_mfma_f32_16x16x32_bf16 v[120:123], v[168:171], v[156:159], v[120:123]\n\t"
    "s_add_u32 s68, s68, 0x2000\n\t"
    "s_cmp_eq_u32 s68, 0x6000\n\t"
    "s_cselect_b32 s68, 0, s68\n\t"
    "v_mfma_f32_16x16x32_bf16 v[124:127], v[172:175], v[156:159], v[124:127]\n\t"
    "s_waitcnt lgkmcnt(3)\n\t"
    "v_mfma_f32_16x16x32_bf16 v[0:3], v[176:179], v[128:131], v[0:3]\n\t"
    "ds_read_b128 v[156:159], v254 offset:26624\n\t"
    "v_mfma_f32_16x16x32_bf16 v[4:7], v[180:183], v[128:131], v[4:7]\n\t"
    "s_waitcnt vmcnt(3)\n\t"
    "v_bitop3_b32 v255, v211, v249, s64 bitop3:0x28\n\t"
    "v_cmp_ne_u32 vcc, 0, v255\n\t"
    "v_mfma_f32_16x16x32_bf16 v[8:11], v[184:187], v[128:131], v[8:11]\n\t"
    "s_cbranch_vccnz .Lslow1_4_%=\n\t"
    ".Lback1_4_%=:\n\t"
    "v_and_b32 v216, s67, v208\n\t"
    "v_lshrrev_b32 v217, 4, v208\n\t"
    "v_mfma_f32_16x16x32_bf16 v[12:15], v[188:191], v[128:131], v[12:15]\n\t"
    "v_and_b32 v218, s67, v209\n\t"
    "s_barrier\n\t"
    "s_setprio 3\n\t"
    "v_mfma_f32_16x16x32_bf16 v[16:19], v[176:179], v[132:135], v[16:19]\n\t"
    "v_lshrrev_b32 v219, 4, v209\n\t"
    "v_and_b32 v220, s67, v210\n\t"
    "ds_read_b128 v[128:131], v253 offset:0\n\t"
    "v_mfma_f32_16x16x32_bf16 v[20:23], v[180:183], v[132:135], v[20:23]\n\t"
    "v_lshrrev_b32 v221, 4, v210\n\t"
    "v_and_b32 v222, s67, v211\n\t"
    "s_add_u32 m0, s61, s68\n\t"
    "v_mfma_f32_16x16x32_bf16 v[24:27], v[184:187], v[132:135], v[24:27]\n\t"
    "v_lshrrev_b32 v223, 4, v211\n\t"
    "v_and_b32 v217, s67, v217\n\t"
    "buffer_load_dwordx4 v236, s[44:47], s60 offen nt lds\n\t"
    "buffer_load_dwordx4 v[208:211], v248, s[48:51], s59 offen sc1\n\t"
    "v_mfma_f32_16x16x32_bf16 v[28:31], v[188:191], v[132:135], v[28:31]\n\t"
    "v_and_b32 v219, s67, v219\n\t"
    "v_and_b32 v221, s67, v221\n\t"
    "v_mfma_f32_16x16x32_bf16 v[32:35], v[176:179], v[136:139], v[32:35]\n\t"
    "v_and_b32 v223, s67, v223\n\t"
    "v_perm_b32 v224, v193, v192, v216\n\t"
    "ds_read_b128 v[132:135], v253 offset:2048\n\t"
    "v_mfma_f32_16x16x32_bf16 v[36:39], v[180:183], v[136:139], v[36:39]\n\t"
    "v_perm_b32 v225, v195, v194, v216\n\t"
    "v_perm_b32 v226, v193, v192, v217\n\t"
    "ds_read2_b32 v[240:241], v244 offset1:112\n\t"
    "v_mfma_f32_16x16x32_bf16 v[40:43], v[184:187], v[136:139], v[40:43]\n\t"
    "v_add_u32 v254, s63, v233\n\t"
    "v_perm_b32 v227, v195, v194, v217\n\t"
    "v_mfma_f32_16x16x32_bf16 v[44:47], v[188:191], v[136:139], v[44:47]\n\t"
    "v_perm_b32 v160, v225, v224, s65\n\t"
    "v_perm_b32 v161, v225, v224, s66\n\t"
    "v_mfma_f32_16x16x32_bf16 v[48:51], v[176:179], v[140:143], v[48:51]\n\t"
    "v_perm_b32 v162, v227, v226, s65\n\t"
    "v_perm_b32 v163, v227, v226, s66\n\t"
    "ds_read_b128 v[136:139], v254 offset:0\n\t"
    "v_mfma_f32_16x16x32_bf16 v[52:55], v[180:183], v[140:143], v[52:55]\n\t"
    "v_perm_b32 v228, v197, v196, v218\n\t"
    "v_perm_b32 v229, v199, v198, v218\n\t"
    "ds_read_b32 v242, v244 offset:1024\n\t"
    "v_mfma_f32_16x16x32_bf16 v[56:59], v[184:187], v[140:143], v[56:59]\n\t"
    "v_perm_b32 v230, v197, v196, v219\n\t"
    "v_perm_b32 v231, v199, v198, v219\n\t"
    "ds_read_b32 v243, v244 offset:1472\n\t"
    "v_mfma_f32_16x16x32_bf16 v[60:63], v[188:191], v[140:143], v[60:63]\n\t"
    "v_perm_b32 v164, v229, v228, s65\n\t"
    "v_perm_b32 v165, v229, v228, s66\n\t"
    "s_waitcnt lgkmcnt(6)\n\t"
    "v_mfma_f32_16x16x32_bf16 v[64:67], v[176:179], v[144:147], v[64:67]\n\t"
    "v_perm_b32 v166, v231, v230, s65\n\t"
    "v_perm_b32 v167, v231, v230, s66\n\t"
    "ds_read_b128 v[140:143], v254 offset:2048\n\t"
    "v_mfma_f32_16x16x32_bf16 v[68:71], v[180:183], v[144:147], v[68:71]\n\t"
    "v_perm_b32 v224, v201, v200, v220\n\t"
    "v_perm_b32 v225, v203, v202, v220\n\t"
    "s_add_u32 m0, s57, 16256\n\t"
    "v_mfma_f32_16x16x32_bf16 v[72:75], v[184:187], v[144:147], v[72:75]\n\t"
    "v_perm_b32 v226, v201, v200, v221\n\t"
    "v_perm_b32 v227, v203, v202, v221\n\t"
    "buffer_load_dwordx4 v234, s[40:43], s56 offen offset:128 lds\n\t"
    "v_mfma_f32_16x16x32_bf16 v[76:79], v[188:191], v[144:147], v[76:79]\n\t"
    "v_perm_b32 v168, v225, v224, s65\n\t"
    "v_perm_b32 v169, v225, v224, s66\n\t"
    "v_mfma_f32_16x16x32_bf16 v[80:83], v[176:179], v[148:151], v[80:83]\n\t"
    "v_perm_b32 v170, v227, v226, s65\n\t"
    "v_perm_b32 v171, v227, v226, s66\n\t"
    "ds_read_b128 v[144:147], v253 offset:8192\n\t"
    "v_mfma_f32_16x16x32_bf16 v[84:87], v[180:183], v[148:151], v[84:87]\n\t"
    "v_perm_b32 v228, v205, v204, v222\n\t"
    "v_perm_b32 v229, v207, v206, v222\n\t"
    "s_add_u32 s60, s60, 0x1c0000\n\t"
    "v_mfma_f32_16x16x32_bf16 v[88:91], v[184:187], v[148:151], v[88:91]\n\t"
    "v_perm_b32 v230, v205, v204, v223\n\t"
    "v_perm_b32 v231, v207, v206, v223\n\t"
    "v_mfma_f32_16x16x32_bf16 v[92:95], v[188:191], v[148:151], v[92:95]\n\t"
    "s_waitcnt lgkmcnt(2)\n\t"
    "v_lshl_or_b32 v240, v241, 8, v240\n\t"
    "v_lshl_or_b32 v242, v243, 8, v242\n\t"
    "v_mfma_f32_16x16x32_bf16 v[96:99], v[176:179], v[152:155], v[96:99]\n\t"
    "v_lshl_or_b32 v240, v242, 16, v240\n\t"
    "v_perm_b32 v172, v229, v228, s65\n\t"
    "ds_read_b128 v[148:151], v253 offset:10240\n\t"
    "v_mfma_f32_16x16x32_bf16 v[100:103], v[180:183], v[152:155], v[100:103]\n\t"
    "v_mov_b32 v245, v240\n\t"
    "v_perm_b32 v173, v229, v228, s66\n\t"
    "s_add_u32 m0, s57, 17280\n\t"
    "v_mfma_f32_16x16x32_bf16 v[104:107], v[184:187], v[152:155], v[104:107]\n\t"
    "v_perm_b32 v174, v231, v230, s65\n\t"
    "v_perm_b32 v175, v231, v230, s66\n\t"
    "buffer_load_dwordx4 v235, s[40:43], s56 offen offset:128 lds\n\t"
    "v_mfma_f32_16x16x32_bf16 v[108:111], v[188:191], v[152:155], v[108:111]\n\t"
    "v_permlane32_swap_b32 v240, v245\n\t"
    "v_mfma_f32_16x16x32_bf16 v[112:115], v[176:179], v[156:159], v[112:115]\n\t"
    "v_lshl_or_b32 v240, v245, 4, v240\n\t"
    "v_or_b32 v240, v240, v239\n\t"
    "ds_read_b128 v[152:155], v254 offset:8192\n\t"
    "v_mfma_f32_16x16x32_bf16 v[116:119], v[180:183], v[156:159], v[116:119]\n\t"
    "buffer_store_dword v240, v238, s[52:55], s62 offen sc0 sc1\n\t"
    "s_add_u32 s62, s62, 0x1000\n\t"
    "s_add_u32 s56, s56, 0x100\n\t"
    "v_mfma_f32_16x16x32_bf16 v[120:123], v[184:187], v[156:159], v[120:123]\n\t"
    "v_mfma_f32_16x16x32_bf16 v[124:127], v[188:191], v[156:159], v[124:127]\n\t"
    "s_cmpk_lt_u32 s56, 0x800\n\t"
    "s_cbranch_scc1 .Lloop_4_%=\n\t"
    "s_mov_b32 s70, 0\n\t"
    "s_lshl_b32 s69, %[wid], 12\n\t"
    "s_waitcnt lgkmcnt(3)\n\t"
    "v_mfma_f32_16x16x32_bf16 v[0:3], v[160:163], v[128:131], v[0:3]\n\t"
    "ds_read_b128 v[156:159], v254 offset:10240\n\t"
    "v_mfma_f32_16x16x32_bf16 v[4:7], v[164:167], v[128:131], v[4:7]\n\t"
    "s_waitcnt vmcnt(7)\n\t"
    "v_bitop3_b32 v255, v215, v250, s64 bitop3:0x28\n\t"
    "v_cmp_ne_u32 vcc, 0, v255\n\t"
    "v_mfma_f32_16x16x32_bf16 v[8:11], v[168:171], v[128:131], v[8:11]\n\t"
    "s_cbranch_vccnz .Lslowa_4_%=\n\t"
    ".Lbacka_4_%=:\n\t"
    "v_and_b32 v216, s67, v212\n\t"
    "v_lshrrev_b32 v217, 4, v212\n\t"
    "v_and_b32 v218, s67, v213\n\t"
    "v_lshrrev_b32 v219, 4, v213\n\t"
    "v_mfma_f32_16x16x32_bf16 v[12:15], v[172:175], v[128:131], v[12:15]\n\t"
    "v_and_b32 v220, s67, v214\n\t"
    "v_lshrrev_b32 v221, 4, v214\n\t"
    "v_and_b32 v222, s67, v215\n\t"
    "v_mfma_f32_16x16x32_bf16 v[16:19], v[160:163], v[132:135], v[16:19]\n\t"
    "v_lshrrev_b32 v223, 4, v215\n\t"
    "v_and_b32 v217, s67, v217\n\t"
    "v_and_b32 v219, s67, v219\n\t"
    "ds_read_b128 v[128:131], v254 offset:16384\n\t"
    "buffer_load_dwordx4 v[212:215], v248, s[48:51], s59 offen offset:2048 sc1\n\t"
    "v_mfma_f32_16x16x32_bf16 v[20:23], v[164:167], v[132:135], v[20:23]\n\t"
    "v_and_b32 v221, s67, v221\n\t"
    "v_and_b32 v223, s67, v223\n\t"
    "v_perm_b32 v224, v193, v192, v216\n\t"
    "s_setprio 1\n\t"
    "v_mfma_f32_16x16x32_bf16 v[24:27], v[168:171], v[132:135], v[24:27]\n\t"
    "v_perm_b32 v225, v195, v194, v216\n\t"
    "v_perm_b32 v226, v193, v192, v217\n\t"
    "v_perm_b32 v227, v195, v194, v217\n\t"
    "v_mfma_f32_16x16x32_bf16 v[28:31], v[172:175], v[132:135], v[28:31]\n\t"
    "v_perm_b32 v176, v225, v224, s65\n\t"
    "v_perm_b32 v177, v225, v224, s66\n\t"
    "v_perm_b32 v178, v227, v226, s65\n\t"
    "v_mfma_f32_16x16x32_bf16 v[32:35], v[160:163], v[136:139], v[32:35]\n\t"
    "v_perm_b32 v179, v227, v226, s66\n\t"
    "v_perm_b32 v228, v197, v196, v218\n\t"
    "v_perm_b32 v229, v199, v198, v218\n\t"
    "ds_read_b128 v[132:135], v253 offset:16384\n\t"
    "v_mfma_f32_16x16x32_bf16 v[36:39], v[164:167], v[136:139], v[36:39]\n\t"
    "v_perm_b32 v230, v197, v196, v219\n\t"
    "v_perm_b32 v231, v199, v198, v219\n\t"
    "v_perm_b32 v180, v229, v228, s65\n\t"
    "v_mfma_f32_16x16x32_bf16 v[40:43], v[168:171], v[136:139], v[40:43]\n\t"
    "v_perm_b32 v181, v229, v228, s66\n\t"
    "v_perm_b32 v182, v231, v230, s65\n\t"
    "v_perm_b32 v183, v231, v230, s66\n\t"
    "v_mfma_f32_16x16x32_bf16 v[44:47], v[172:175], v[136:139], v[44:47]\n\t"
    "v_perm_b32 v224, v201, v200, v220\n\t"
    "v_perm_b32 v225, v203, v202, v220\n\t"
    "v_perm_b32 v226, v201, v200, v221\n\t"
    "v_perm_b32 v227, v203, v202, v221\n\t"
    "s_waitcnt vmcnt(1)\n\t"
    "s_barrier\n\t"
    "v_mfma_f32_16x16x32_bf16 v[48:51], v[160:163], v[140:143], v[48:51]\n\t"
    "v_perm_b32 v184, v225, v224, s65\n\t"
    "v_perm_b32 v185, v225, v224, s66\n\t"
    "v_perm_b32 v186, v227, v226, s65\n\t"
    "ds_read_b128 v[136:139], v254 offset:32768\n\t"
    "v_mfma_f32_16x16x32_bf16 v[52:55], v[164:167], v[140:143], v[52:55]\n\t"
    "v_perm_b32 v187, v227, v226, s66\n\t"
    "v_perm_b32 v228, v205, v204, v222\n\t"
    "v_perm_b32 v229, v207, v206, v222\n\t"
    "v_mfma_f32_16x16x32_bf16 v[56:59], v[168:171], v[140:143], v[56:59]\n\t"
    "v_perm_b32 v230, v205, v204, v223\n\t"
    "v_perm_b32 v231, v207, v206, v223\n\t"
    "v_perm_b32 v188, v229, v228, s65\n\t"
    "v_mfma_f32_16x16x32_bf16 v[60:63], v[172:175], v[140:143], v[60:63]\n\t"
    "v_perm_b32 v189, v229, v228, s66\n\t"
    "v_perm_b32 v190, v231, v230, s65\n\t"
    "v_perm_b32 v191, v231, v230, s66\n\t"
    "s_waitcnt lgkmcnt(3)\n\t"
    "v_mfma_f32_16x16x32_bf16 v[64:67], v[160:163], v[144:147], v[64:67]\n\t"
    "s_waitcnt vmcnt(4)\n\t"
    "v_bitop3_b32 v255, v211, v249, s64 bitop3:0x28\n\t"
    "v_cmp_ne_u32 vcc, 0, v255\n\t"
    "s_cbranch_vccnz .Lslowb_4_%=\n\t"
    ".Lbackb_4_%=:\n\t"
    "v_and_b32 v216, s67, v208\n\t"
    "ds_read_b128 v[140:143], v253 offset:32768\n\t"
    "v_mfma_f32_16x16x32_bf16 v[68:71], v[164:167], v[144:147], v[68:71]\n\t"
    "v_lshrrev_b32 v217, 4, v208\n\t"
    "v_and_b32 v218, s67, v209\n\t"
    "v_lshrrev_b32 v219, 4, v209\n\t"
    "v_mfma_f32_16x16x32_bf16 v[72:75], v[168:171], v[144:147], v[72:75]\n\t"
    "v_and_b32 v220, s67, v210\n\t"
    "v_lshrrev_b32 v221, 4, v210\n\t"
    "v_and_b32 v222, s67, v211\n\t"
    "v_mfma_f32_16x16x32_bf16 v[76:79], v[172:175], v[144:147], v[76:79]\n\t"
    "v_lshrrev_b32 v223, 4, v211\n\t"
    "v_and_b32 v217, s67, v217\n\t"
    "v_and_b32 v219, s67, v219\n\t"
    "v_mfma_f32_16x16x32_bf16 v[80:83], v[160:163], v[148:151], v[80:83]\n\t"
    "v_and_b32 v221, s67, v221\n\t"
    "v_and_b32 v223, s67, v223\n\t"
    "v_perm_b32 v224, v193, v192, v216\n\t"
    "ds_read_b128 v[144:147], v254 offset:18432\n\t"
    "v_mfma_f32_16x16x32_bf16 v[84:87], v[164:167], v[148:151], v[84:87]\n\t"
    "v_perm_b32 v225, v195, v194, v216\n\t"
    "v_perm_b32 v226, v193, v192, v217\n\t"
    "v_perm_b32 v227, v195, v194, v217\n\t"
    "v_perm_b32 v232, v225, v224, s65\n\t"
    "v_mfma_f32_16x16x32_bf16 v[88:91], v[168:171], v[148:151], v[88:91]\n\t"
    "v_perm_b32 v233, v225, v224, s66\n\t"
    "v_perm_b32 v234, v227, v226, s65\n\t"
    "v_perm_b32 v235, v227, v226, s66\n\t"
    "v_mfma_f32_16x16x32_bf16 v[92:95], v[172:175], v[148:151], v[92:95]\n\t"
    "v_perm_b32 v228, v197, v196, v218\n\t"
    "v_perm_b32 v229, v199, v198, v218\n\t"
    "v_perm_b32 v230, v197, v196, v219\n\t"
    "v_mfma_f32_16x16x32_bf16 v[96:99], v[160:163], v[152:155], v[96:99]\n\t"
    "v_perm_b32 v231, v199, v198, v219\n\t"
    "v_perm_b32 v236, v229, v228, s65\n\t"
    "v_perm_b32 v237, v229, v228, s66\n\t"
    "ds_read_b128 v[148:151], v254 offset:34816\n\t"
    "v_mfma_f32_16x16x32_bf16 v[100:103], v[164:167], v[152:155], v[100:103]\n\t"
    "v_perm_b32 v238, v231, v230, s65\n\t"
    "v_perm_b32 v239, v231, v230, s66\n\t"
    "v_perm_b32 v224, v201, v200, v220\n\t"
    "v_mfma_f32_16x16x32_bf16 v[104:107], v[168:171], v[152:155], v[104:107]\n\t"
    "v_perm_b32 v225, v203, v202, v220\n\t"
    "v_perm_b32 v226, v201, v200, v221\n\t"
    "v_perm_b32 v227, v203, v202, v221\n\t"
    "v_mfma_f32_16x16x32_bf16 v[108:111], v[172:175], v[152:155], v[108:111]\n\t"
    "v_perm_b32 v240, v225, v224, s65\n\t"
    "v_perm_b32 v241, v225, v224, s66\n\t"
    "v_perm_b32 v242, v227, v226, s65\n\t"
    "v_mfma_f32_16x16x32_bf16 v[112:115], v[160:163], v[156:159], v[112:115]\n\t"
    "v_perm_b32 v243, v227, v226, s66\n\t"
    "v_perm_b32 v228, v205, v204, v222\n\t"
    "v_perm_b32 v229, v207, v206, v222\n\t"
    "ds_read_b128 v[152:155], v254 offset:49152\n\t"
    "v_mfma_f32_16x16x32_bf16 v[116:119], v[164:167], v[156:159], v[116:119]\n\t"
    "v_perm_b32 v230, v205, v204, v223\n\t"
    "v_perm_b32 v231, v207, v206, v223\n\t"
    "v_perm_b32 v244, v229, v228, s65\n\t"
    "v_mfma_f32_16x16x32_bf16 v[120:123], v[168:171], v[156:159], v[120:123]\n\t"
    "v_perm_b32 v245, v229, v228, s66\n\t"
    "v_perm_b32 v246, v231, v230, s65\n\t"
    "v_perm_b32 v247, v231, v230, s66\n\t"
    "v_mfma_f32_16x16x32_bf16 v[124:127], v[172:175], v[156:159], v[124:127]\n\t"
    "s_waitcnt lgkmcnt(5)\n\t"
    "v_mfma_f32_16x16x32_bf16 v[32:35], v[176:179], v[128:131], v[32:35]\n\t"
    "ds_read_b128 v[156:159], v253 offset:49152\n\t"
    "v_mfma_f32_16x16x32_bf16 v[36:39], v[180:183], v[128:131], v[36:39]\n\t"
    "s_waitcnt vmcnt(0)\n\t"
    "v_bitop3_b32 v255, v215, v250, s64 bitop3:0x28\n\t"
    "v_cmp_ne_u32 vcc, 0, v255\n\t"
    "v_mfma_f32_16x16x32_bf16 v[40:43], v[184:187], v[128:131], v[40:43]\n\t"
    "s_cbranch_vccnz .Lslowc_4_%=\n\t"
    ".Lbackc_4_%=:\n\t"
    "v_and_b32 v216, s67, v212\n\t"
    "v_lshrrev_b32 v217, 4, v212\n\t"
    "v_and_b32 v218, s67, v213\n\t"
    "v_mfma_f32_16x16x32_bf16 v[44:47], v[188:191], v[128:131], v[44:47]\n\t"
    "v_lshrrev_b32 v219, 4, v213\n\t"
    "v_and_b32 v220, s67, v214\n\t"
    "v_mfma_f32_16x16x32_bf16 v[0:3], v[176:179], v[132:135], v[0:3]\n\t"
    "v_lshrrev_b32 v221, 4, v214\n\t"
    "v_and_b32 v222, s67, v215\n\t"
    "ds_read_b128 v[128:131], v254 offset:51200\n\t"
    "v_mfma_f32_16x16x32_bf16 v[4:7], v[180:183], v[132:135], v[4:7]\n\t"
    "v_lshrrev_b32 v223, 4, v215\n\t"
    "v_and_b32 v217, s67, v217\n\t"
    "v_mfma_f32_16x16x32_bf16 v[8:11], v[184:187], v[132:135], v[8:11]\n\t"
    "v_and_b32 v219, s67, v219\n\t"
    "v_and_b32 v221, s67, v221\n\t"
    "v_mfma_f32_16x16x32_bf16 v[12:15], v[188:191], v[132:135], v[12:15]\n\t"
    "v_and_b32 v223, s67, v223\n\t"
    "v_perm_b32 v224, v193, v192, v216\n\t"
    "s_waitcnt lgkmcnt(5)\n\t"
    "v_mfma_f32_16x16x32_bf16 v[32:35], v[232:235], v[136:139], v[32:35]\n\t"
    "v_perm_b32 v225, v195, v194, v216\n\t"
    "v_perm_b32 v226, v193, v192, v217\n\t"
    "ds_read_b128 v[132:135], v253 offset:18432\n\t"
    "v_mfma_f32_16x16x32_bf16 v[36:39], v[236:239], v[136:139], v[36:39]\n\t"
    "v_perm_b32 v227, v195, v194, v217\n\t"
    "v_perm_b32 v160, v225, v224, s65\n\t"
    "v_mfma_f32_16x16x32_bf16 v[40:43], v[240:243], v[136:139], v[40:43]\n\t"
    "v_perm_b32 v161, v225, v224, s66\n\t"
    "v_perm_b32 v162, v227, v226, s65\n\t"
    "v_mfma_f32_16x16x32_bf16 v[44:47], v[244:247], v[136:139], v[44:47]\n\t"
    "v_perm_b32 v163, v227, v226, s66\n\t"
    "v_perm_b32 v228, v197, v196, v218\n\t"
    "v_mfma_f32_16x16x32_bf16 v[0:3], v[232:235], v[140:143], v[0:3]\n\t"
    "v_perm_b32 v229, v199, v198, v218\n\t"
    "v_perm_b32 v230, v197, v196, v219\n\t"
    "v_perm_b32 v231, v199, v198, v219\n\t"
    "ds_read_b128 v[136:139], v253 offset:34816\n\t"
    "v_mfma_f32_16x16x32_bf16 v[4:7], v[236:239], v[140:143], v[4:7]\n\t"
    "v_perm_b32 v164, v229, v228, s65\n\t"
    "v_perm_b32 v165, v229, v228, s66\n\t"
    "v_mfma_f32_16x16x32_bf16 v[8:11], v[240:243], v[140:143], v[8:11]\n\t"
    "v_perm_b32 v166, v231, v230, s65\n\t"
    "v_perm_b32 v167, v231, v230, s66\n\t"
    "v_mfma_f32_16x16x32_bf16 v[12:15], v[244:247], v[140:143], v[12:15]\n\t"
    "v_perm_b32 v224, v201, v200, v220\n\t"
    "v_perm_b32 v225, v203, v202, v220\n\t"
    "s_waitcnt lgkmcnt(5)\n\t"
    "v_mfma_f32_16x16x32_bf16 v[48:51], v[176:179], v[144:147], v[48:51]\n\t"
    "v_perm_b32 v226, v201, v200, v221\n\t"
    "v_perm_b32 v227, v203, v202, v221\n\t"
    "ds_read_b128 v[140:143], v253 offset:51200\n\t"
    "v_mfma_f32_16x16x32_bf16 v[52:55], v[180:183], v[144:147], v[52:55]\n\t"
    "v_perm_b32 v168, v225, v224, s65\n\t"
    "v_perm_b32 v169, v225, v224, s66\n\t"
    "v_mfma_f32_16x16x32_bf16 v[56:59], v[184:187], v[144:147], v[56:59]\n\t"
    "v_perm_b32 v170, v227, v226, s65\n\t"
    "v_perm_b32 v171, v227, v226, s66\n\t"
    "v_mfma_f32_16x16x32_bf16 v[60:63], v[188:191], v[144:147], v[60:63]\n\t"
    "v_perm_b32 v228, v205, v204, v222\n\t"
    "v_perm_b32 v229, v207, v206, v222\n\t"
    "v_mfma_f32_16x16x32_bf16 v[48:51], v[232:235], v[148:151], v[48:51]\n\t"
    "v_perm_b32 v230, v205, v204, v223\n\t"
    "v_perm_b32 v231, v207, v206, v223\n\t"
    "ds_read_b128 v[144:147], v254 offset:24576\n\t"
    "v_mfma_f32_16x16x32_bf16 v[52:55], v[236:239], v[148:151], v[52:55]\n\t"
    "v_perm_b32 v172, v229, v228, s65\n\t"
    "v_perm_b32 v173, v229, v228, s66\n\t"
    "v_mfma_f32_16x16x32_bf16 v[56:59], v[240:243], v[148:151], v[56:59]\n\t"
    "v_perm_b32 v174, v231, v230, s65\n\t"
    "v_perm_b32 v175, v231, v230, s66\n\t"
    "v_mfma_f32_16x16x32_bf16 v[60:63], v[244:247], v[148:151], v[60:63]\n\t"
    "s_waitcnt lgkmcnt(5)\n\t"
    "v_mfma_f32_16x16x32_bf16 v[32:35], v[160:163], v[152:155], v[32:35]\n\t"
    "v_add_u32 v216, s69, v248\n\t"
    "v_xor_b32 v217, 0x1000, v216\n\t"
    "ds_read_b128 v[148:151], v254 offset:40960\n\t"
    "v_mfma_f32_16x16x32_bf16 v[36:39], v[164:167], v[152:155], v[36:39]\n\t"
    "v_and_b32 v218, 0xffff, v251\n\t"
    "v_lshrrev_b32 v219, 16, v251\n\t"
    "v_mfma_f32_16x16x32_bf16 v[40:43], v[168:171], v[152:155], v[40:43]\n\t"
    "v_add_u32 v218, 131072, v218\n\t"
    "v_add_u32 v219, 131072, v219\n\t"
    "v_mfma_f32_16x16x32_bf16 v[44:47], v[172:175], v[152:155], v[44:47]\n\t"
    "v_mfma_f32_16x16x32_bf16 v[0:3], v[160:163], v[156:159], v[0:3]\n\t"
    "ds_read_b128 v[152:155], v254 offset:57344\n\t"
    "v_mfma_f32_16x16x32_bf16 v[4:7], v[164:167], v[156:159], v[4:7]\n\t"
    "v_mfma_f32_16x16x32_bf16 v[8:11], v[168:171], v[156:159], v[8:11]\n\t"
    "ds_write_b128 v216, v[32:35] offset:0\n\t"
    "v_mfma_f32_16x16x32_bf16 v[12:15], v[172:175], v[156:159], v[12:15]\n\t"
    "ds_write_b128 v216, v[36:39] offset:1024\n\t"
    "s_waitcnt lgkmcnt(7)\n\t"
    "v_mfma_f32_16x16x32_bf16 v[48:51], v[160:163], v[128:131], v[48:51]\n\t"
    "ds_read_b128 v[156:159], v253 offset:24576\n\t"
    "ds_write_b128 v216, v[40:43] offset:2048\n\t"
    "v_mfma_f32_16x16x32_bf16 v[52:55], v[164:167], v[128:131], v[52:55]\n\t"
    "ds_write_b128 v216, v[44:47] offset:3072\n\t"
    "v_mfma_f32_16x16x32_bf16 v[56:59], v[168:171], v[128:131], v[56:59]\n\t"
    "v_mfma_f32_16x16x32_bf16 v[60:63], v[172:175], v[128:131], v[60:63]\n\t"
    "s_waitcnt lgkmcnt(0)\n\t"
    "s_barrier\n\t"
    "v_mfma_f32_16x16x32_bf16 v[16:19], v[176:179], v[132:135], v[16:19]\n\t"
    "ds_read_b128 v[128:131], v253 offset:40960\n\t"
    "s_cmp_lg_u32 %[wid], 0\n\t"
    "s_cbranch_scc1 .Lnoat_4_%=\n\t"
    "s_mov_b64 exec, 1\n\t"
    "v_mov_b32 v255, 0\n\t"
    "v_mov_b32 v220, 1\n\t"
    "global_atomic_add v255, v220, %[ctr] sc1\n\t"
    "s_mov_b64 exec, -1\n\t"
    "s_nop 3\n\t"
    ".Lnoat_4_%=:\n\t"
    "ds_read_b128 v[32:35], v217 offset:0\n\t"
    "v_mfma_f32_16x16x32_bf16 v[20:23], v[180:183], v[132:135], v[20:23]\n\t"
    "ds_read_b128 v[36:39], v217 offset:1024\n\t"
    "v_mfma_f32_16x16x32_bf16 v[24:27], v[184:187], v[132:135], v[24:27]\n\t"
    "ds_read_b128 v[40:43], v217 offset:2048\n\t"
    "v_mfma_f32_16x16x32_bf16 v[28:31], v[188:191], v[132:135], v[28:31]\n\t"
    "ds_read_b128 v[44:47], v217 offset:3072\n\t"
    "ds_write_b128 v216, v[48:51] offset:32768\n\t"
    "s_waitcnt lgkmcnt(14)\n\t"
    "v_mfma_f32_16x16x32_bf16 v[16:19], v[232:235], v[136:139], v[16:19]\n\t"
    "s_waitcnt lgkmcnt(4)\n\t"
    "v_add_f32 v32, v0, v32\n\t"
    "v_add_f32 v33, v1, v33\n\t"
    "ds_read_b128 v[132:135], v253 offset:57344\n\t"
    "ds_write_b128 v216, v[52:55] offset:33792\n\t"
    "v_mfma_f32_16x16x32_bf16 v[20:23], v[236:239], v[136:139], v[20:23]\n\t"
    "v_add_f32 v34, v2, v34\n\t"
    "v_add_f32 v35, v3, v35\n\t"
    "ds_write_b128 v216, v[56:59] offset:34816\n\t"
    "v_mfma_f32_16x16x32_bf16 v[24:27], v[240:243], v[136:139], v[24:27]\n\t"
    "v_cvt_pk_bf16_f32 v32, v32, v33\n\t"
    "v_cvt_pk_bf16_f32 v33, v34, v35\n\t"
    "ds_write_b64 v218, v[32:33] offset:0\n\t"
    "s_waitcnt lgkmcnt(7)\n\t"
    "ds_write_b128 v216, v[60:63] offset:35840\n\t"
    "v_mfma_f32_16x16x32_bf16 v[28:31], v[244:247], v[136:139], v[28:31]\n\t"
    "v_add_f32 v36, v4, v36\n\t"
    "v_add_f32 v37, v5, v37\n\t"
    "v_mfma_f32_16x16x32_bf16 v[16:19], v[160:163], v[140:143], v[16:19]\n\t"
    "v_add_f32 v38, v6, v38\n\t"
    "v_add_f32 v39, v7, v39\n\t"
    "ds_read_b128 v[136:139], v254 offset:26624\n\t"
    "v_mfma_f32_16x16x32_bf16 v[20:23], v[164:167], v[140:143], v[20:23]\n\t"
    "v_cvt_pk_bf16_f32 v36, v36, v37\n\t"
    "v_cvt_pk_bf16_f32 v37, v38, v39\n\t"
    "ds_write_b64 v218, v[36:37] offset:32\n\t"
    "s_waitcnt lgkmcnt(9)\n\t"
    "v_mfma_f32_16x16x32_bf16 v[24:27], v[168:171], v[140:143], v[24:27]\n\t"
    "v_add_f32 v40, v8, v40\n\t"
    "v_add_f32 v41, v9, v41\n\t"
    "v_mfma_f32_16x16x32_bf16 v[28:31], v[172:175], v[140:143], v[28:31]\n\t"
    "v_add_f32 v42, v10, v42\n\t"
    "v_add_f32 v43, v11, v43\n\t"
    "s_waitcnt lgkmcnt(15)\n\t"
    "v_mfma_f32_16x16x32_bf16 v[96:99], v[176:179], v[144:147], v[96:99]\n\t"
    "v_cvt_pk_bf16_f32 v40, v40, v41\n\t"
    "v_cvt_pk_bf16_f32 v41, v42, v43\n\t"
    "ds_write_b64 v218, v[40:41] offset:64\n\t"
    "s_waitcnt lgkmcnt(9)\n\t"
    "ds_read_b128 v[140:143], v254 offset:43008\n\t"
    "v_mfma_f32_16x16x32_bf16 v[100:103], v[180:183], v[144:147], v[100:103]\n\t"
    "v_add_f32 v44, v12, v44\n\t"
    "v_add_f32 v45, v13, v45\n\t"
    "v_mfma_f32_16x16x32_bf16 v[104:107], v[184:187], v[144:147], v[104:107]\n\t"
    "v_add_f32 v46, v14, v46\n\t"
    "v_add_f32 v47, v15, v47\n\t"
    "v_mfma_f32_16x16x32_bf16 v[108:111], v[188:191], v[144:147], v[108:111]\n\t"
    "v_cvt_pk_bf16_f32 v44, v44, v45\n\t"
    "v_cvt_pk_bf16_f32 v45, v46, v47\n\t"
    "ds_write_b64 v218, v[44:45] offset:96\n\t"
    "v_mfma_f32_16x16x32_bf16 v[96:99], v[232:235], v[148:151], v[96:99]\n\t"
    "ds_read_b128 v[144:147], v254 offset:59392\n\t"
    "s_waitcnt lgkmcnt(6)\n\t"
    "s_waitcnt lgkmcnt(1)\n\t"
    "s_barrier\n\t"
    "v_mfma_f32_16x16x32_bf16 v[100:103], v[236:239], v[148:151], v[100:103]\n\t"
    "ds_read_b128 v[224:227], v219 offset:0\n\t"
    "ds_read_b128 v[228:231], v219 offset:928\n\t"
    "v_mfma_f32_16x16x32_bf16 v[104:107], v[240:243], v[148:151], v[104:107]\n\t"
    "ds_read_b128 v[48:51], v217 offset:32768\n\t"
    "v_mfma_f32_16x16x32_bf16 v[108:111], v[244:247], v[148:151], v[108:111]\n\t"
    "ds_read_b128 v[52:55], v217 offset:33792\n\t"
    "ds_read_b128 v[56:59], v217 offset:34816\n\t"
    "s_waitcnt lgkmcnt(15)\n\t"
    "v_mfma_f32_16x16x32_bf16 v[96:99], v[160:163], v[152:155], v[96:99]\n\t"
    "ds_read_b128 v[148:151], v253 offset:26624\n\t"
    "ds_read_b128 v[60:63], v217 offset:35840\n\t"
    "v_mfma_f32_16x16x32_bf16 v[100:103], v[164:167], v[152:155], v[100:103]\n\t"
    "s_waitcnt lgkmcnt(4)\n\t"
    "v_add_f32 v48, v16, v48\n\t"
    "v_add_f32 v49, v17, v49\n\t"
    "s_waitcnt lgkmcnt(6)\n\t"
    "s_add_u32 s71, s70, 0\n\t"
    "v_mfma_f32_16x16x32_bf16 v[104:107], v[168:171], v[152:155], v[104:107]\n\t"
    "v_add_f32 v50, v18, v50\n\t"
    "v_add_f32 v51, v19, v51\n\t"
    "buffer_store_dwordx4 v[224:227], v252, s[72:75], s71 offen sc1\n\t"
    "v_mfma_f32_16x16x32_bf16 v[108:111], v[172:175], v[152:155], v[108:111]\n\t"
    "v_cvt_pk_bf16_f32 v48, v48, v49\n\t"
    "v_cvt_pk_bf16_f32 v49, v50, v51\n\t"
    "ds_write_b64 v218, v[48:49] offset:14848\n\t"
    "s_waitcnt lgkmcnt(4)\n\t"
    "s_waitcnt lgkmcnt(6)\n\t"
    "s_add_u32 s71, s70, 14336\n\t"
    "v_mfma_f32_16x16x32_bf16 v[64:67], v[176:179], v[156:159], v[64:67]\n\t"
    "v_add_f32 v52, v20, v52\n\t"
    "v_add_f32 v53, v21, v53\n\t"
    "ds_read_b128 v[152:155], v253 offset:43008\n\t"
    "buffer_store_dwordx4 v[228:231], v252, s[72:75], s71 offen sc1\n\t"
    "v_mfma_f32_16x16x32_bf16 v[68:71], v[180:183], v[156:159], v[68:71]\n\t"
    "v_add_f32 v54, v22, v54\n\t"
    "v_add_f32 v55, v23, v55\n\t"
    "v_mfma_f32_16x16x32_bf16 v[72:75], v[184:187], v[156:159], v[72:75]\n\t"
    "v_cvt_pk_bf16_f32 v52, v52, v53\n\t"
    "v_cvt_pk_bf16_f32 v53, v54, v55\n\t"
    "ds_write_b64 v218, v[52:53] offset:14880\n\t"
    "s_waitcnt lgkmcnt(5)\n\t"
    "v_mfma_f32_16x16x32_bf16 v[76:79], v[188:191], v[156:159], v[76:79]\n\t"
    "v_add_f32 v56, v24, v56\n\t"
    "v_add_f32 v57, v25, v57\n\t"
    "ds_write_b128 v216, v[96:99] offset:0\n\t"
    "s_waitcnt lgkmcnt(15)\n\t"
    "v_mfma_f32_16x16x32_bf16 v[64:67], v[232:235], v[128:131], v[64:67]\n\t"
    "v_add_f32 v58, v26, v58\n\t"
    "v_add_f32 v59, v27, v59\n\t"
    "ds_read_b128 v[156:159], v253 offset:59392\n\t"
    "ds_write_b128 v216, v[100:103] offset:1024\n\t"
    "v_mfma_f32_16x16x32_bf16 v[68:71], v[236:239], v[128:131], v[68:71]\n\t"
    "v_cvt_pk_bf16_f32 v56, v56, v57\n\t"
    "v_cvt_pk_bf16_f32 v57, v58, v59\n\t"
    "ds_write_b64 v218, v[56:57] offset:14912\n\t"
    "s_waitcnt lgkmcnt(7)\n\t"
    "v_mfma_f32_16x16x32_bf16 v[72:75], v[240:243], v[128:131], v[72:75]\n\t"
    "v_add_f32 v60, v28, v60\n\t"
    "v_add_f32 v61, v29, v61\n\t"
    "ds_write_b128 v216, v[104:107] offset:2048\n\t"
    "v_mfma_f32_16x16x32_bf16 v[76:79], v[244:247], v[128:131], v[76:79]\n\t"
    "v_add_f32 v62, v30, v62\n\t"
    "v_add_f32 v63, v31, v63\n\t"
    "ds_write_b128 v216, v[108:111] offset:3072\n\t"
    "v_mfma_f32_16x16x32_bf16 v[64:67], v[160:163], v[132:135], v[64:67]\n\t"
    "v_cvt_pk_bf16_f32 v60, v60, v61\n\t"
    "v_cvt_pk_bf16_f32 v61, v62, v63\n\t"
    "ds_write_b64 v218, v[60:61] offset:14944\n\t"
    "v_mfma_f32_16x16x32_bf16 v[68:71], v[164:167], v[132:135], v[68:71]\n\t"
    "s_waitcnt lgkmcnt(1)\n\t"
    "s_waitcnt lgkmcnt(0)\n\t"
    "s_barrier\n\t"
    "v_mfma_f32_16x16x32_bf16 v[72:75], v[168:171], v[132:135], v[72:75]\n\t"
    "ds_read_b128 v[224:227], v219 offset:14848\n\t"
    "ds_read_b128 v[228:231], v219 offset:15776\n\t"
    "v_mfma_f32_16x16x32_bf16 v[76:79], v[172:175], v[132:135], v[76:79]\n\t"
    "ds_read_b128 v[96:99], v217 offset:0\n\t"
    "s_waitcnt lgkmcnt(15)\n\t"
    "v_mfma_f32_16x16x32_bf16 v[112:115], v[176:179], v[136:139], v[112:115]\n\t"
    "ds_read_b128 v[100:103], v217 offset:1024\n\t"
    "ds_read_b128 v[104:107], v217 offset:2048\n\t"
    "v_mfma_f32_16x16x32_bf16 v[116:119], v[180:183], v[136:139], v[116:119]\n\t"
    "ds_read_b128 v[108:111], v217 offset:3072\n\t"
    "v_mfma_f32_16x16x32_bf16 v[120:123], v[184:187], v[136:139], v[120:123]\n\t"
    "s_waitcnt lgkmcnt(5)\n\t"
    "s_add_u32 s71, s70, 114688\n\t"
    "v_mfma_f32_16x16x32_bf16 v[124:127], v[188:191], v[136:139], v[124:127]\n\t"
    "buffer_store_dwordx4 v[224:227], v252, s[72:75], s71 offen sc1\n\t"
    "v_mfma_f32_16x16x32_bf16 v[112:115], v[232:235], v[140:143], v[112:115]\n\t"
    "s_waitcnt lgkmcnt(3)\n\t"
    "v_add_f32 v96, v64, v96\n\t"
    "v_add_f32 v97, v65, v97\n\t"
    "s_waitcnt lgkmcnt(4)\n\t"
    "s_add_u32 s71, s70, 129024\n\t"
    "v_mfma_f32_16x16x32_bf16 v[116:119], v[236:239], v[140:143], v[116:119]\n\t"
    "v_add_f32 v98, v66, v98\n\t"
    "v_add_f32 v99, v67, v99\n\t"
    "buffer_store_dwordx4 v[228:231], v252, s[72:75], s71 offen sc1\n\t"
    "v_mfma_f32_16x16x32_bf16 v[120:123], v[240:243], v[140:143], v[120:123]\n\t"
    "v_cvt_pk_bf16_f32 v96, v96, v97\n\t"
    "v_cvt_pk_bf16_f32 v97, v98, v99\n\t"
    "ds_write_b64 v218, v[96:97] offset:0\n\t"
    "s_waitcnt lgkmcnt(3)\n\t"
    "v_mfma_f32_16x16x32_bf16 v[124:127], v[244:247], v[140:143], v[124:127]\n\t"
    "v_add_f32 v100, v68, v100\n\t"
    "v_add_f32 v101, v69, v101\n\t"
    "s_waitcnt lgkmcnt(15)\n\t"
    "v_mfma_f32_16x16x32_bf16 v[112:115], v[160:163], v[144:147], v[112:115]\n\t"
    "v_add_f32 v102, v70, v102\n\t"
    "v_add_f32 v103, v71, v103\n\t"
    "v_mfma_f32_16x16x32_bf16 v[116:119], v[164:167], v[144:147], v[116:119]\n\t"
    "v_cvt_pk_bf16_f32 v100, v100, v101\n\t"
    "v_cvt_pk_bf16_f32 v101, v102, v103\n\t"
    "ds_write_b64 v218, v[100:101] offset:32\n\t"
    "s_waitcnt lgkmcnt(3)\n\t"
    "v_mfma_f32_16x16x32_bf16 v[120:123], v[168:171], v[144:147], v[120:123]\n\t"
    "v_add_f32 v104, v72, v104\n\t"
    "v_add_f32 v105, v73, v105\n\t"
    "v_mfma_f32_16x16x32_bf16 v[124:127], v[172:175], v[144:147], v[124:127]\n\t"
    "v_add_f32 v106, v74, v106\n\t"
    "v_add_f32 v107, v75, v107\n\t"
    "v_mfma_f32_16x16x32_bf16 v[80:83], v[176:179], v[148:151], v[80:83]\n\t"
    "v_cvt_pk_bf16_f32 v104, v104, v105\n\t"
    "v_cvt_pk_bf16_f32 v105, v106, v107\n\t"
    "ds_write_b64 v218, v[104:105] offset:64\n\t"
    "s_waitcnt lgkmcnt(3)\n\t"
    "v_mfma_f32_16x16x32_bf16 v[84:87], v[180:183], v[148:151], v[84:87]\n\t"
    "v_add_f32 v108, v76, v108\n\t"
    "v_add_f32 v109, v77, v109\n\t"
    "v_mfma_f32_16x16x32_bf16 v[88:91], v[184:187], v[148:151], v[88:91]\n\t"
    "v_add_f32 v110, v78, v110\n\t"
    "v_add_f32 v111, v79, v111\n\t"
    "ds_write_b128 v216, v[112:115] offset:32768\n\t"
    "v_mfma_f32_16x16x32_bf16 v[92:95], v[188:191], v[148:151], v[92:95]\n\t"
    "v_cvt_pk_bf16_f32 v108, v108, v109\n\t"
    "v_cvt_pk_bf16_f32 v109, v110, v111\n\t"
    "ds_write_b64 v218, v[108:109] offset:96\n\t"
    "ds_write_b128 v216, v[116:119] offset:33792\n\t"
    "s_waitcnt lgkmcnt(15)\n\t"
    "v_mfma_f32_16x16x32_bf16 v[80:83], v[232:235], v[152:155], v[80:83]\n\t"
    "ds_write_b128 v216, v[120:123] offset:34816\n\t"
    "v_mfma_f32_16x16x32_bf16 v[84:87], v[236:239], v[152:155], v[84:87]\n\t"
    "ds_write_b128 v216, v[124:127] offset:35840\n\t"
    "v_mfma_f32_16x16x32_bf16 v[88:91], v[240:243], v[152:155], v[88:91]\n\t"
    "v_mfma_f32_16x16x32_bf16 v[92:95], v[244:247], v[152:155], v[92:95]\n\t"
    "s_waitcnt lgkmcnt(0)\n\t"
    "s_waitcnt lgkmcnt(3)\n\t"
    "s_barrier\n\t"
    "v_mfma_f32_16x16x32_bf16 v[80:83], v[160:163], v[156:159], v[80:83]\n\t"
    "ds_read_b128 v[224:227], v219 offset:0\n\t"
    "ds_read_b128 v[228:231], v219 offset:928\n\t"
    "v_mfma_f32_16x16x32_bf16 v[84:87], v[164:167], v[156:159], v[84:87]\n\t"
    "ds_read_b128 v[112:115], v217 offset:32768\n\t"
    "v_mfma_f32_16x16x32_bf16 v[88:91], v[168:171], v[156:159], v[88:91]\n\t"
    "ds_read_b128 v[116:119], v217 offset:33792\n\t"
    "ds_read_b128 v[120:123], v217 offset:34816\n\t"
    "v_mfma_f32_16x16x32_bf16 v[92:95], v[172:175], v[156:159], v[92:95]\n\t"
    "ds_read_b128 v[124:127], v217 offset:35840\n\t"
    "s_setprio 0\n\t"
    "s_waitcnt lgkmcnt(5)\n\t"
    "s_add_u32 s71, s70, 458752\n\t"
    "buffer_store_dwordx4 v[224:227], v252, s[72:75], s71 offen sc1\n\t"
    "s_waitcnt lgkmcnt(4)\n\t"
    "s_add_u32 s71, s70, 473088\n\t"
    "buffer_store_dwordx4 v[228:231], v252, s[72:75], s71 offen sc1\n\t"
    "s_nop 3\n\t"
    "s_waitcnt lgkmcnt(3)\n\t"
    "v_add_f32 v112, v80, v112\n\t"
    "v_add_f32 v113, v81, v113\n\t"
    "v_add_f32 v114, v82, v114\n\t"
    "v_add_f32 v115, v83, v115\n\t"
    "v_cvt_pk_bf16_f32 v112, v112, v113\n\t"
    "v_cvt_pk_bf16_f32 v113, v114, v115\n\t"
    "ds_write_b64 v218, v[112:113] offset:14848\n\t"
    "s_waitcnt lgkmcnt(3)\n\t"
    "v_add_f32 v116, v84, v116\n\t"
    "v_add_f32 v117, v85, v117\n\t"
    "v_add_f32 v118, v86, v118\n\t"
    "v_add_f32 v119, v87, v119\n\t"
    "v_cvt_pk_bf16_f32 v116, v116, v117\n\t"
    "v_cvt_pk_bf16_f32 v117, v118, v119\n\t"
    "ds_write_b64 v218, v[116:117] offset:14880\n\t"
    "s_waitcnt lgkmcnt(3)\n\t"
    "v_add_f32 v120, v88, v120\n\t"
    "v_add_f32 v121, v89, v121\n\t"
    "v_add_f32 v122, v90, v122\n\t"
    "v_add_f32 v123, v91, v123\n\t"
    "v_cvt_pk_bf16_f32 v120, v120, v121\n\t"
    "v_cvt_pk_bf16_f32 v121, v122, v123\n\t"
    "ds_write_b64 v218, v[120:121] offset:14912\n\t"
    "s_waitcnt lgkmcnt(3)\n\t"
    "v_add_f32 v124, v92, v124\n\t"
    "v_add_f32 v125, v93, v125\n\t"
    "v_add_f32 v126, v94, v126\n\t"
    "v_add_f32 v127, v95, v127\n\t"
    "v_cvt_pk_bf16_f32 v124, v124, v125\n\t"
    "v_cvt_pk_bf16_f32 v125, v126, v127\n\t"
    "ds_write_b64 v218, v[124:125] offset:14944\n\t"
    "s_waitcnt lgkmcnt(0)\n\t"
    "s_barrier\n\t"
    "ds_read_b128 v[224:227], v219 offset:14848\n\t"
    "ds_read_b128 v[228:231], v219 offset:15776\n\t"
    "s_waitcnt lgkmcnt(1)\n\t"
    "s_add_u32 s71, s70, 573440\n\t"
    "buffer_store_dwordx4 v[224:227], v252, s[72:75], s71 offen sc1\n\t"
    "s_waitcnt lgkmcnt(0)\n\t"
    "s_add_u32 s71, s70, 587776\n\t"
    "buffer_store_dwordx4 v[228:231], v252, s[72:75], s71 offen sc1\n\t"
    "s_branch .Lend_4_%=\n\t"
    ".Lslow0_4_%=:\n\t"
    "s_add_i32 s71, s59, -2048\n\t"
    "s_movk_i32 s70, 0x7fff\n\t"
    ".Lretry0_4_%=:\n\t"
    "s_sleep 1\n\t"
    "buffer_load_dwordx4 v[212:215], v248, s[48:51], s71 offen sc1\n\t"
    "s_waitcnt vmcnt(0)\n\t"
    "v_bitop3_b32 v255, v215, v250, s64 bitop3:0x28\n\t"
    "v_cmp_ne_u32 vcc, 0, v255\n\t"
    "s_cbranch_vccz .Lback0_4_%=\n\t"
    "s_sub_u32 s70, s70, 1\n\t"
    "s_cbranch_scc0 .Lretry0_4_%=\n\t"
    "s_branch .Lback0_4_%=\n\t"
    ".Lslow1_4_%=:\n\t"
    "s_add_i32 s71, s59, -4096\n\t"
    "s_movk_i32 s70, 0x7fff\n\t"
    ".Lretry1_4_%=:\n\t"
    "s_sleep 1\n\t"
    "buffer_load_dwordx4 v[208:211], v248, s[48:51], s71 offen sc1\n\t"
    "s_waitcnt vmcnt(0)\n\t"
    "v_bitop3_b32 v255, v211, v249, s64 bitop3:0x28\n\t"
    "v_cmp_ne_u32 vcc, 0, v255\n\t"
    "s_cbranch_vccz .Lback1_4_%=\n\t"
    "s_sub_u32 s70, s70, 1\n\t"
    "s_cbranch_scc0 .Lretry1_4_%=\n\t"
    "s_branch .Lback1_4_%=\n\t"
    ".Lslowa_4_%=:\n\t"
    "s_add_i32 s71, s59, -2048\n\t"
    "s_movk_i32 s57, 0x7fff\n\t"
    ".Lretrya_4_%=:\n\t"
    "s_sleep 1\n\t"
    "buffer_load_dwordx4 v[212:215], v248, s[48:51], s71 offen sc1\n\t"
    "s_waitcnt vmcnt(0)\n\t"
    "v_bitop3_b32 v255, v215, v250, s64 bitop3:0x28\n\t"
    "v_cmp_ne_u32 vcc, 0, v255\n\t"
    "s_cbranch_vccz .Lbacka_4_%=\n\t"
    "s_sub_u32 s57, s57, 1\n\t"
    "s_cbranch_scc0 .Lretrya_4_%=\n\t"
    "s_branch .Lbacka_4_%=\n\t"
    ".Lslowb_4_%=:\n\t"
    "s_add_i32 s71, s59, 0\n\t"
    "s_movk_i32 s57, 0x7fff\n\t"
    ".Lretryb_4_%=:\n\t"
    "s_sleep 1\n\t"
    "buffer_load_dwordx4 v[208:211], v248, s[48:51], s71 offen sc1\n\t"
    "s_waitcnt vmcnt(0)\n\t"
    "v_bitop3_b32 v255, v211, v249, s64 bitop3:0x28\n\t"
    "v_cmp_ne_u32 vcc, 0, v255\n\t"
    "s_cbranch_vccz .Lbackb_4_%=\n\t"
    "s_sub_u32 s57, s57, 1\n\t"
    "s_cbranch_scc0 .Lretryb_4_%=\n\t"
    "s_branch .Lbackb_4_%=\n\t"
    ".Lslowc_4_%=:\n\t"
    "s_add_i32 s71, s59, 2048\n\t"
    "s_movk_i32 s57, 0x7fff\n\t"
    ".Lretryc_4_%=:\n\t"
    "s_sleep 1\n\t"
    "buffer_load_dwordx4 v[212:215], v248, s[48:51], s71 offen sc1\n\t"
    "s_waitcnt vmcnt(0)\n\t"
    "v_bitop3_b32 v255, v215, v250, s64 bitop3:0x28\n\t"
    "v_cmp_ne_u32 vcc, 0, v255\n\t"
    "s_cbranch_vccz .Lbackc_4_%=\n\t"
    "s_sub_u32 s57, s57, 1\n\t"
    "s_cbranch_scc0 .Lretryc_4_%=\n\t"
    "s_branch .Lbackc_4_%=\n\t"
    ".Lend_4_%=:\n\t"
    "s_branch .Lalldone_%=\n\t"
    ".Lrole3_%=:\n\t"
    "s_mov_b32 s58, %[xk]\n\t"
    "s_mov_b32 s61, %[ql]\n\t"
    "s_mov_b32 s62, %[so]\n\t"
    "s_movk_i32 s56, 0x200\n\t"
    "s_mov_b32 s60, 0x700000\n\t"
    "s_movk_i32 s59, 0x1000\n\t"
    "s_mov_b32 s63, 0\n\t"
    "s_mov_b32 s64, 0x88888888\n\t"
    "s_mov_b32 s65, 0x05010400\n\t"
    "s_mov_b32 s66, 0x07030602\n\t"
    "s_mov_b32 s67, 0x07070707\n\t"
    "s_mov_b32 s68, 0\n\t"
    "v_mov_b32 v253, v232\n\t"
    "v_mov_b32 v254, v233\n\t"
    "ds_read_b128 v[128:131], v253 offset:0\n\t"
    "ds_read_b128 v[132:135], v253 offset:2048\n\t"
    "ds_read_b128 v[136:139], v254 offset:0\n\t"
    "ds_read_b128 v[140:143], v254 offset:2048\n\t"
    "ds_read_b128 v[144:147], v253 offset:8192\n\t"
    "ds_read_b128 v[148:151], v253 offset:10240\n\t"
    "ds_read_b128 v[152:155], v254 offset:8192\n\t"
    "v_and_b32 v216, s67, v208\n\t"
    "v_lshrrev_b32 v217, 4, v208\n\t"
    "v_and_b32 v218, s67, v209\n\t"
    "v_lshrrev_b32 v219, 4, v209\n\t"
    "v_and_b32 v220, s67, v210\n\t"
    "v_lshrrev_b32 v221, 4, v210\n\t"
    "buffer_load_dwordx4 v[208:211], v248, s[48:51], s59 offen sc1\n\t"
    "v_and_b32 v217, s67, v217\n\t"
    "v_and_b32 v219, s67, v219\n\t"
    "v_and_b32 v221, s67, v221\n\t"
    "v_perm_b32 v224, v193, v192, v216\n\t"
    "v_perm_b32 v225, v195, v194, v216\n\t"
    "v_perm_b32 v226, v193, v192, v217\n\t"
    "v_perm_b32 v227, v195, v194, v217\n\t"
    "v_perm_b32 v160, v225, v224, s65\n\t"
    "v_perm_b32 v161, v225, v224, s66\n\t"
    "v_perm_b32 v162, v227, v226, s65\n\t"
    "v_perm_b32 v163, v227, v226, s66\n\t"
    "v_perm_b32 v228, v197, v196, v218\n\t"
    "v_perm_b32 v229, v199, v198, v218\n\t"
    "v_perm_b32 v230, v197, v196, v219\n\t"
    "v_perm_b32 v231, v199, v198, v219\n\t"
    "v_perm_b32 v164, v229, v228, s65\n\t"
    "v_perm_b32 v165, v229, v228, s66\n\t"
    "v_perm_b32 v166, v231, v230, s65\n\t"
    "v_perm_b32 v167, v231, v230, s66\n\t"
    "v_perm_b32 v224, v201, v200, v220\n\t"
    "v_perm_b32 v225, v203, v202, v220\n\t"
    "v_perm_b32 v226, v201, v200, v221\n\t"
    "v_perm_b32 v227, v203, v202, v221\n\t"
    "v_perm_b32 v168, v225, v224, s65\n\t"
    "v_perm_b32 v169, v225, v224, s66\n\t"
    "v_perm_b32 v170, v227, v226, s65\n\t"
    "v_perm_b32 v171, v227, v226, s66\n\t"
    "s_setprio 2\n\t"
    "s_nop 1\n\t"
    ".Lloop_3_%=:\n\t"
    "s_waitcnt lgkmcnt(3)\n\t"
    "v_mfma_f32_16x16x32_bf16 v[0:3], v[160:163], v[128:131], v[0:3]\n\t"
    "ds_read_b128 v[156:159], v254 offset:10240\n\t"
    "v_mfma_f32_16x16x32_bf16 v[4:7], v[164:167], v[128:131], v[4:7]\n\t"
    "s_waitcnt vmcnt(7)\n\t"
    "v_bitop3_b32 v255, v215, v250, s64 bitop3:0x28\n\t"
    "v_cmp_ne_u32 vcc, 0, v255\n\t"
    "v_mfma_f32_16x16x32_bf16 v[8:11], v[168:171], v[128:131], v[8:11]\n\t"
    "s_cbranch_vccnz .Lslow0_3_%=\n\t"
    ".Lback0_3_%=:\n\t"
    "v_and_b32 v216, s67, v212\n\t"
    "v_lshrrev_b32 v217, 4, v212\n\t"
    "v_mfma_f32_16x16x32_bf16 v[16:19], v[160:163], v[132:135], v[16:19]\n\t"
    "v_and_b32 v218, s67, v213\n\t"
    "v_lshrrev_b32 v219, 4, v213\n\t"
    "ds_read_b128 v[128:131], v253 offset:16384\n\t"
    "v_mfma_f32_16x16x32_bf16 v[20:23], v[164:167], v[132:135], v[20:23]\n\t"
    "v_and_b32 v220, s67, v214\n\t"
    "v_lshrrev_b32 v221, 4, v214\n\t"
    "s_xor_b32 s57, s63, s58\n\t"
    "buffer_load_dwordx4 v[212:215], v248, s[48:51], s59 offen offset:2048 sc1\n\t"
    "v_mfma_f32_16x16x32_bf16 v[24:27], v[168:171], v[132:135], v[24:27]\n\t"
    "v_and_b32 v217, s67, v217\n\t"
    "v_and_b32 v219, s67, v219\n\t"
    "v_mfma_f32_16x16x32_bf16 v[32:35], v[160:163], v[136:139], v[32:35]\n\t"
    "v_and_b32 v221, s67, v221\n\t"
    "v_perm_b32 v224, v193, v192, v216\n\t"
    "ds_read_b128 v[132:135], v253 offset:18432\n\t"
    "v_mfma_f32_16x16x32_bf16 v[36:39], v[164:167], v[136:139], v[36:39]\n\t"
    "v_perm_b32 v225, v195, v194, v216\n\t"
    "s_add_u32 s63, s63, 0x8000\n\t"
    "s_and_b32 s63, s63, 0x18000\n\t"
    "v_mfma_f32_16x16x32_bf16 v[40:43], v[168:171], v[136:139], v[40:43]\n\t"
    "v_perm_b32 v226, v193, v192, v217\n\t"
    "v_perm_b32 v227, v195, v194, v217\n\t"
    "s_setprio 2\n\t"
    "v_mfma_f32_16x16x32_bf16 v[48:51], v[160:163], v[140:143], v[48:51]\n\t"
    "v_perm_b32 v176, v225, v224, s65\n\t"
    "v_perm_b32 v177, v225, v224, s66\n\t"
    "ds_read_b128 v[136:139], v254 offset:16384\n\t"
    "v_mfma_f32_16x16x32_bf16 v[52:55], v[164:167], v[140:143], v[52:55]\n\t"
    "v_perm_b32 v178, v227, v226, s65\n\t"
    "v_perm_b32 v179, v227, v226, s66\n\t"
    "v_mfma_f32_16x16x32_bf16 v[56:59], v[168:171], v[140:143], v[56:59]\n\t"
    "v_perm_b32 v228, v197, v196, v218\n\t"
    "v_perm_b32 v229, v199, v198, v218\n\t"
    "s_waitcnt lgkmcnt(3)\n\t"
    "v_mfma_f32_16x16x32_bf16 v[64:67], v[160:163], v[144:147], v[64:67]\n\t"
    "v_perm_b32 v230, v197, v196, v219\n\t"
    "v_perm_b32 v231, v199, v198, v219\n\t"
    "ds_read_b128 v[140:143], v254 offset:18432\n\t"
    "v_mfma_f32_16x16x32_bf16 v[68:71], v[164:167], v[144:147], v[68:71]\n\t"
    "v_perm_b32 v180, v229, v228, s65\n\t"
    "v_perm_b32 v181, v229, v228, s66\n\t"
    "s_add_u32 m0, s57, 0\n\t"
    "v_mfma_f32_16x16x32_bf16 v[72:75], v[168:171], v[144:147], v[72:75]\n\t"
    "v_perm_b32 v182, v231, v230, s65\n\t"
    "v_perm_b32 v183, v231, v230, s66\n\t"
    "buffer_load_dwordx4 v234, s[40:43], s56 offen lds\n\t"
    "v_mfma_f32_16x16x32_bf16 v[80:83], v[160:163], v[148:151], v[80:83]\n\t"
    "v_perm_b32 v224, v201, v200, v220\n\t"
    "v_perm_b32 v225, v203, v202, v220\n\t"
    "ds_read_b128 v[144:147], v253 offset:24576\n\t"
    "v_mfma_f32_16x16x32_bf16 v[84:87], v[164:167], v[148:151], v[84:87]\n\t"
    "v_perm_b32 v226, v201, v200, v221\n\t"
    "v_perm_b32 v227, v203, v202, v221\n\t"
    "s_add_u32 s59, s59, 0x1000\n\t"
    "v_mfma_f32_16x16x32_bf16 v[88:91], v[168:171], v[148:151], v[88:91]\n\t"
    "v_perm_b32 v184, v225, v224, s65\n\t"
    "v_perm_b32 v185, v225, v224, s66\n\t"
    "s_setprio 1\n\t"
    "v_mfma_f32_16x16x32_bf16 v[96:99], v[160:163], v[152:155], v[96:99]\n\t"
    "v_perm_b32 v186, v227, v226, s65\n\t"
    "v_perm_b32 v187, v227, v226, s66\n\t"
    "ds_read_b128 v[148:151], v253 offset:26624\n\t"
    "v_mfma_f32_16x16x32_bf16 v[100:103], v[164:167], v[152:155], v[100:103]\n\t"
    "s_add_u32 m0, s57, 1024\n\t"
    "v_mfma_f32_16x16x32_bf16 v[104:107], v[168:171], v[152:155], v[104:107]\n\t"
    "buffer_load_dwordx4 v235, s[40:43], s56 offen lds\n\t"
    "v_mfma_f32_16x16x32_bf16 v[112:115], v[160:163], v[156:159], v[112:115]\n\t"
    "ds_read_b128 v[152:155], v254 offset:24576\n\t"
    "s_setprio 0\n\t"
    "v_mfma_f32_16x16x32_bf16 v[116:119], v[164:167], v[156:159], v[116:119]\n\t"
    "v_add_u32 v253, s63, v232\n\t"
    "v_add_u32 v244, s68, v237\n\t"
    "v_mfma_f32_16x16x32_bf16 v[120:123], v[168:171], v[156:159], v[120:123]\n\t"
    "s_add_u32 s68, s68, 0x2000\n\t"
    "s_cmp_eq_u32 s68, 0x6000\n\t"
    "s_cselect_b32 s68, 0, s68\n\t"
    "s_waitcnt lgkmcnt(3)\n\t"
    "v_mfma_f32_16x16x32_bf16 v[0:3], v[176:179], v[128:131], v[0:3]\n\t"
    "ds_read_b128 v[156:159], v254 offset:26624\n\t"
    "v_mfma_f32_16x16x32_bf16 v[4:7], v[180:183], v[128:131], v[4:7]\n\t"
    "s_waitcnt vmcnt(3)\n\t"
    "v_bitop3_b32 v255, v211, v249, s64 bitop3:0x28\n\t"
    "v_cmp_ne_u32 vcc, 0, v255\n\t"
    "v_mfma_f32_16x16x32_bf16 v[8:11], v[184:187], v[128:131], v[8:11]\n\t"
    "s_cbranch_vccnz .Lslow1_3_%=\n\t"
    ".Lback1_3_%=:\n\t"
    "v_and_b32 v216, s67, v208\n\t"
    "s_barrier\n\t"
    "s_setprio 3\n\t"
    "v_mfma_f32_16x16x32_bf16 v[16:19], v[176:179], v[132:135], v[16:19]\n\t"
    "v_lshrrev_b32 v217, 4, v208\n\t"
    "v_and_b32 v218, s67, v209\n\t"
    "ds_read_b128 v[128:131], v253 offset:0\n\t"
    "v_mfma_f32_16x16x32_bf16 v[20:23], v[180:183], v[132:135], v[20:23]\n\t"
    "v_lshrrev_b32 v219, 4, v209\n\t"
    "v_and_b32 v220, s67, v210\n\t"
    "s_add_u32 m0, s61, s68\n\t"
    "v_mfma_f32_16x16x32_bf16 v[24:27], v[184:187], v[132:135], v[24:27]\n\t"
    "v_lshrrev_b32 v221, 4, v210\n\t"
    "v_and_b32 v217, s67, v217\n\t"
    "buffer_load_dwordx4 v236, s[44:47], s60 offen nt lds\n\t"
    "buffer_load_dwordx4 v[208:211], v248, s[48:51], s59 offen sc1\n\t"
    "v_mfma_f32_16x16x32_bf16 v[32:35], v[176:179], v[136:139], v[32:35]\n\t"
    "v_and_b32 v219, s67, v219\n\t"
    "v_and_b32 v221, s67, v221\n\t"
    "ds_read_b128 v[132:135], v253 offset:2048\n\t"
    "v_mfma_f32_16x16x32_bf16 v[36:39], v[180:183], v[136:139], v[36:39]\n\t"
    "v_perm_b32 v224, v193, v192, v216\n\t"
    "v_perm_b32 v225, v195, v194, v216\n\t"
    "ds_read2_b32 v[240:241], v244 offset1:112\n\t"
    "v_mfma_f32_16x16x32_bf16 v[40:43], v[184:187], v[136:139], v[40:43]\n\t"
    "v_add_u32 v254, s63, v233\n\t"
    "v_perm_b32 v226, v193, v192, v217\n\t"
    "v_mfma_f32_16x16x32_bf16 v[48:51], v[176:179], v[140:143], v[48:51]\n\t"
    "v_perm_b32 v227, v195, v194, v217\n\t"
    "v_perm_b32 v160, v225, v224, s65\n\t"
    "ds_read_b128 v[136:139], v254 offset:0\n\t"
    "v_mfma_f32_16x16x32_bf16 v[52:55], v[180:183], v[140:143], v[52:55]\n\t"
    "v_perm_b32 v161, v225, v224, s66\n\t"
    "v_perm_b32 v162, v227, v226, s65\n\t"
    "ds_read_b32 v242, v244 offset:1024\n\t"
    "v_mfma_f32_16x16x32_bf16 v[56:59], v[184:187], v[140:143], v[56:59]\n\t"
    "v_perm_b32 v163, v227, v226, s66\n\t"
    "v_perm_b32 v228, v197, v196, v218\n\t"
    "ds_read_b32 v243, v244 offset:1472\n\t"
    "s_waitcnt lgkmcnt(6)\n\t"
    "v_mfma_f32_16x16x32_bf16 v[64:67], v[176:179], v[144:147], v[64:67]\n\t"
    "v_perm_b32 v229, v199, v198, v218\n\t"
    "v_perm_b32 v230, v197, v196, v219\n\t"
    "ds_read_b128 v[140:143], v254 offset:2048\n\t"
    "v_mfma_f32_16x16x32_bf16 v[68:71], v[180:183], v[144:147], v[68:71]\n\t"
    "v_perm_b32 v231, v199, v198, v219\n\t"
    "v_perm_b32 v164, v229, v228, s65\n\t"
    "s_add_u32 m0, s57, 16256\n\t"
    "v_mfma_f32_16x16x32_bf16 v[72:75], v[184:187], v[144:147], v[72:75]\n\t"
    "v_perm_b32 v165, v229, v228, s66\n\t"
    "v_perm_b32 v166, v231, v230, s65\n\t"
    "buffer_load_dwordx4 v234, s[40:43], s56 offen offset:128 lds\n\t"
    "v_mfma_f32_16x16x32_bf16 v[80:83], v[176:179], v[148:151], v[80:83]\n\t"
    "v_perm_b32 v167, v231, v230, s66\n\t"
    "v_perm_b32 v224, v201, v200, v220\n\t"
    "ds_read_b128 v[144:147], v253 offset:8192\n\t"
    "v_mfma_f32_16x16x32_bf16 v[84:87], v[180:183], v[148:151], v[84:87]\n\t"
    "v_perm_b32 v225, v203, v202, v220\n\t"
    "v_perm_b32 v226, v201, v200, v221\n\t"
    "s_add_u32 s60, s60, 0x1c0000\n\t"
    "v_mfma_f32_16x16x32_bf16 v[88:91], v[184:187], v[148:151], v[88:91]\n\t"
    "s_waitcnt lgkmcnt(2)\n\t"
    "v_lshl_or_b32 v240, v241, 8, v240\n\t"
    "v_lshl_or_b32 v242, v243, 8, v242\n\t"
    "v_mfma_f32_16x16x32_bf16 v[96:99], v[176:179], v[152:155], v[96:99]\n\t"
    "v_lshl_or_b32 v240, v242, 16, v240\n\t"
    "v_perm_b32 v227, v203, v202, v221\n\t"
    "ds_read_b128 v[148:151], v253 offset:10240\n\t"
    "v_mfma_f32_16x16x32_bf16 v[100:103], v[180:183], v[152:155], v[100:103]\n\t"
    "v_mov_b32 v245, v240\n\t"
    "v_perm_b32 v168, v225, v224, s65\n\t"
    "s_add_u32 m0, s57, 17280\n\t"
    "v_mfma_f32_16x16x32_bf16 v[104:107], v[184:187], v[152:155], v[104:107]\n\t"
    "v_perm_b32 v169, v225, v224, s66\n\t"
    "v_perm_b32 v170, v227, v226, s65\n\t"
    "buffer_load_dwordx4 v235, s[40:43], s56 offen offset:128 lds\n\t"
    "v_mfma_f32_16x16x32_bf16 v[112:115], v[176:179], v[156:159], v[112:115]\n\t"
    "v_permlane32_swap_b32 v240, v245\n\t"
    "v_perm_b32 v171, v227, v226, s66\n\t"
    "ds_read_b128 v[152:155], v254 offset:8192\n\t"
    "v_mfma_f32_16x16x32_bf16 v[116:119], v[180:183], v[156:159], v[116:119]\n\t"
    "v_lshl_or_b32 v240, v245, 4, v240\n\t"
    "v_or_b32 v240, v240, v239\n\t"
    "v_mfma_f32_16x16x32_bf16 v[120:123], v[184:187], v[156:159], v[120:123]\n\t"
    "buffer_store_dword v240, v238, s[52:55], s62 offen sc0 sc1\n\t"
    "s_add_u32 s62, s62, 0x1000\n\t"
    "s_add_u32 s56, s56, 0x100\n\t"
    "s_cmpk_lt_u32 s56, 0x800\n\t"
    "s_cbranch_scc1 .Lloop_3_%=\n\t"
    "s_mov_b32 s70, 229376\n\t"
    "s_lshl_b32 s69, %[wid], 12\n\t"
    "s_waitcnt lgkmcnt(3)\n\t"
    "v_mfma_f32_16x16x32_bf16 v[0:3], v[160:163], v[128:131], v[0:3]\n\t"
    "ds_read_b128 v[156:159], v254 offset:10240\n\t"
    "v_mfma_f32_16x16x32_bf16 v[4:7], v[164:167], v[128:131], v[4:7]\n\t"
    "s_waitcnt vmcnt(7)\n\t"
    "v_bitop3_b32 v255, v215, v250, s64 bitop3:0x28\n\t"
    "v_cmp_ne_u32 vcc, 0, v255\n\t"
    "v_mfma_f32_16x16x32_bf16 v[8:11], v[168:171], v[128:131], v[8:11]\n\t"
    "s_cbranch_vccnz .Lslowa_3_%=\n\t"
    ".Lbacka_3_%=:\n\t"
    "v_and_b32 v216, s67, v212\n\t"
    "v_lshrrev_b32 v217, 4, v212\n\t"
    "v_and_b32 v218, s67, v213\n\t"
    "v_lshrrev_b32 v219, 4, v213\n\t"
    "v_mfma_f32_16x16x32_bf16 v[16:19], v[160:163], v[132:135], v[16:19]\n\t"
    "v_and_b32 v220, s67, v214\n\t"
    "v_lshrrev_b32 v221, 4, v214\n\t"
    "v_and_b32 v217, s67, v217\n\t"
    "ds_read_b128 v[128:131], v254 offset:16384\n\t"
    "buffer_load_dwordx4 v[212:215], v248, s[48:51], s59 offen offset:2048 sc1\n\t"
    "v_mfma_f32_16x16x32_bf16 v[20:23], v[164:167], v[132:135], v[20:23]\n\t"
    "v_and_b32 v219, s67, v219\n\t"
    "v_and_b32 v221, s67, v221\n\t"
    "v_perm_b32 v224, v193, v192, v216\n\t"
    "s_setprio 1\n\t"
    "v_mfma_f32_16x16x32_bf16 v[24:27], v[168:171], v[132:135], v[24:27]\n\t"
    "v_perm_b32 v225, v195, v194, v216\n\t"
    "v_perm_b32 v226, v193, v192, v217\n\t"
    "v_perm_b32 v227, v195, v194, v217\n\t"
    "v_mfma_f32_16x16x32_bf16 v[32:35], v[160:163], v[136:139], v[32:35]\n\t"
    "v_perm_b32 v176, v225, v224, s65\n\t"
    "v_perm_b32 v177, v225, v224, s66\n\t"
    "v_perm_b32 v178, v227, v226, s65\n\t"
    "v_perm_b32 v179, v227, v226, s66\n\t"
    "ds_read_b128 v[132:135], v253 offset:16384\n\t"
    "v_mfma_f32_16x16x32_bf16 v[36:39], v[164:167], v[136:139], v[36:39]\n\t"
    "v_perm_b32 v228, v197, v196, v218\n\t"
    "v_perm_b32 v229, v199, v198, v218\n\t"
    "v_perm_b32 v230, v197, v196, v219\n\t"
    "v_mfma_f32_16x16x32_bf16 v[40:43], v[168:171], v[136:139], v[40:43]\n\t"
    "v_perm_b32 v231, v199, v198, v219\n\t"
    "v_perm_b32 v180, v229, v228, s65\n\t"
    "v_perm_b32 v181, v229, v228, s66\n\t"
    "s_waitcnt vmcnt(1)\n\t"
    "s_barrier\n\t"
    "v_mfma_f32_16x16x32_bf16 v[48:51], v[160:163], v[140:143], v[48:51]\n\t"
    "v_perm_b32 v182, v231, v230, s65\n\t"
    "v_perm_b32 v183, v231, v230, s66\n\t"
    "v_perm_b32 v224, v201, v200, v220\n\t"
    "ds_read_b128 v[136:139], v254 offset:32768\n\t"
    "v_mfma_f32_16x16x32_bf16 v[52:55], v[164:167], v[140:143], v[52:55]\n\t"
    "v_perm_b32 v225, v203, v202, v220\n\t"
    "v_perm_b32 v226, v201, v200, v221\n\t"
    "v_perm_b32 v227, v203, v202, v221\n\t"
    "v_perm_b32 v184, v225, v224, s65\n\t"
    "v_mfma_f32_16x16x32_bf16 v[56:59], v[168:171], v[140:143], v[56:59]\n\t"
    "v_perm_b32 v185, v225, v224, s66\n\t"
    "v_perm_b32 v186, v227, v226, s65\n\t"
    "v_perm_b32 v187, v227, v226, s66\n\t"
    "s_waitcnt lgkmcnt(3)\n\t"
    "v_mfma_f32_16x16x32_bf16 v[64:67], v[160:163], v[144:147], v[64:67]\n\t"
    "s_waitcnt vmcnt(4)\n\t"
    "v_bitop3_b32 v255, v211, v249, s64 bitop3:0x28\n\t"
    "v_cmp_ne_u32 vcc, 0, v255\n\t"
    "s_cbranch_vccnz .Lslowb_3_%=\n\t"
    ".Lbackb_3_%=:\n\t"
    "v_and_b32 v216, s67, v208\n\t"
    "ds_read_b128 v[140:143], v253 offset:32768\n\t"
    "v_mfma_f32_16x16x32_bf16 v[68:71], v[164:167], v[144:147], v[68:71]\n\t"
    "v_lshrrev_b32 v217, 4, v208\n\t"
    "v_and_b32 v218, s67, v209\n\t"
    "v_lshrrev_b32 v219, 4, v209\n\t"
    "v_mfma_f32_16x16x32_bf16 v[72:75], v[168:171], v[144:147], v[72:75]\n\t"
    "v_and_b32 v220, s67, v210\n\t"
    "v_lshrrev_b32 v221, 4, v210\n\t"
    "v_and_b32 v217, s67, v217\n\t"
    "v_and_b32 v219, s67, v219\n\t"
    "v_mfma_f32_16x16x32_bf16 v[80:83], v[160:163], v[148:151], v[80:83]\n\t"
    "v_and_b32 v221, s67, v221\n\t"
    "v_perm_b32 v224, v193, v192, v216\n\t"
    "v_perm_b32 v225, v195, v194, v216\n\t"
    "ds_read_b128 v[144:147], v254 offset:18432\n\t"
    "v_mfma_f32_16x16x32_bf16 v[84:87], v[164:167], v[148:151], v[84:87]\n\t"
    "v_perm_b32 v226, v193, v192, v217\n\t"
    "v_perm_b32 v227, v195, v194, v217\n\t"
    "v_perm_b32 v232, v225, v224, s65\n\t"
    "v_mfma_f32_16x16x32_bf16 v[88:91], v[168:171], v[148:151], v[88:91]\n\t"
    "v_perm_b32 v233, v225, v224, s66\n\t"
    "v_perm_b32 v234, v227, v226, s65\n\t"
    "v_perm_b32 v235, v227, v226, s66\n\t"
    "v_mfma_f32_16x16x32_bf16 v[96:99], v[160:163], v[152:155], v[96:99]\n\t"
    "v_perm_b32 v228, v197, v196, v218\n\t"
    "v_perm_b32 v229, v199, v198, v218\n\t"
    "v_perm_b32 v230, v197, v196, v219\n\t"
    "v_perm_b32 v231, v199, v198, v219\n\t"
    "ds_read_b128 v[148:151], v254 offset:34816\n\t"
    "v_mfma_f32_16x16x32_bf16 v[100:103], v[164:167], v[152:155], v[100:103]\n\t"
    "v_perm_b32 v236, v229, v228, s65\n\t"
    "v_perm_b32 v237, v229, v228, s66\n\t"
    "v_perm_b32 v238, v231, v230, s65\n\t"
    "v_mfma_f32_16x16x32_bf16 v[104:107], v[168:171], v[152:155], v[104:107]\n\t"
    "v_perm_b32 v239, v231, v230, s66\n\t"
    "v_perm_b32 v224, v201, v200, v220\n\t"
    "v_perm_b32 v225, v203, v202, v220\n\t"
    "v_mfma_f32_16x16x32_bf16 v[112:115], v[160:163], v[156:159], v[112:115]\n\t"
    "v_perm_b32 v226, v201, v200, v221\n\t"
    "v_perm_b32 v227, v203, v202, v221\n\t"
    "v_perm_b32 v240, v225, v224, s65\n\t"
    "ds_read_b128 v[152:155], v254 offset:49152\n\t"
    "v_mfma_f32_16x16x32_bf16 v[116:119], v[164:167], v[156:159], v[116:119]\n\t"
    "v_perm_b32 v241, v225, v224, s66\n\t"
    "v_perm_b32 v242, v227, v226, s65\n\t"
    "v_perm_b32 v243, v227, v226, s66\n\t"
    "v_mfma_f32_16x16x32_bf16 v[120:123], v[168:171], v[156:159], v[120:123]\n\t"
    "s_waitcnt lgkmcnt(5)\n\t"
    "v_mfma_f32_16x16x32_bf16 v[32:35], v[176:179], v[128:131], v[32:35]\n\t"
    "ds_read_b128 v[156:159], v253 offset:49152\n\t"
    "v_mfma_f32_16x16x32_bf16 v[36:39], v[180:183], v[128:131], v[36:39]\n\t"
    "s_waitcnt vmcnt(0)\n\t"
    "v_bitop3_b32 v255, v215, v250, s64 bitop3:0x28\n\t"
    "v_cmp_ne_u32 vcc, 0, v255\n\t"
    "v_mfma_f32_16x16x32_bf16 v[40:43], v[184:187], v[128:131], v[40:43]\n\t"
    "s_cbranch_vccnz .Lslowc_3_%=\n\t"
    ".Lbackc_3_%=:\n\t"
    "v_and_b32 v216, s67, v212\n\t"
    "v_lshrrev_b32 v217, 4, v212\n\t"
    "v_and_b32 v218, s67, v213\n\t"
    "v_mfma_f32_16x16x32_bf16 v[0:3], v[176:179], v[132:135], v[0:3]\n\t"
    "v_lshrrev_b32 v219, 4, v213\n\t"
    "v_and_b32 v220, s67, v214\n\t"
    "ds_read_b128 v[128:131], v254 offset:51200\n\t"
    "v_mfma_f32_16x16x32_bf16 v[4:7], v[180:183], v[132:135], v[4:7]\n\t"
    "v_lshrrev_b32 v221, 4, v214\n\t"
    "v_and_b32 v217, s67, v217\n\t"
    "v_mfma_f32_16x16x32_bf16 v[8:11], v[184:187], v[132:135], v[8:11]\n\t"
    "v_and_b32 v219, s67, v219\n\t"
    "v_and_b32 v221, s67, v221\n\t"
    "s_waitcnt lgkmcnt(5)\n\t"
    "v_mfma_f32_16x16x32_bf16 v[32:35], v[232:235], v[136:139], v[32:35]\n\t"
    "v_perm_b32 v224, v193, v192, v216\n\t"
    "v_perm_b32 v225, v195, v194, v216\n\t"
    "ds_read_b128 v[132:135], v253 offset:18432\n\t"
    "v_mfma_f32_16x16x32_bf16 v[36:39], v[236:239], v[136:139], v[36:39]\n\t"
    "v_perm_b32 v226, v193, v192, v217\n\t"
    "v_perm_b32 v227, v195, v194, v217\n\t"
    "v_perm_b32 v160, v225, v224, s65\n\t"
    "v_mfma_f32_16x16x32_bf16 v[40:43], v[240:243], v[136:139], v[40:43]\n\t"
    "v_perm_b32 v161, v225, v224, s66\n\t"
    "v_perm_b32 v162, v227, v226, s65\n\t"
    "v_mfma_f32_16x16x32_bf16 v[0:3], v[232:235], v[140:143], v[0:3]\n\t"
    "v_perm_b32 v163, v227, v226, s66\n\t"
    "v_perm_b32 v228, v197, v196, v218\n\t"
    "ds_read_b128 v[136:139], v253 offset:34816\n\t"
    "v_mfma_f32_16x16x32_bf16 v[4:7], v[236:239], v[140:143], v[4:7]\n\t"
    "v_perm_b32 v229, v199, v198, v218\n\t"
    "v_perm_b32 v230, v197, v196, v219\n\t"
    "v_mfma_f32_16x16x32_bf16 v[8:11], v[240:243], v[140:143], v[8:11]\n\t"
    "v_perm_b32 v231, v199, v198, v219\n\t"
    "v_perm_b32 v164, v229, v228, s65\n\t"
    "s_waitcnt lgkmcnt(5)\n\t"
    "v_mfma_f32_16x16x32_bf16 v[48:51], v[176:179], v[144:147], v[48:51]\n\t"
    "v_perm_b32 v165, v229, v228, s66\n\t"
    "v_perm_b32 v166, v231, v230, s65\n\t"
    "v_perm_b32 v167, v231, v230, s66\n\t"
    "ds_read_b128 v[140:143], v253 offset:51200\n\t"
    "v_mfma_f32_16x16x32_bf16 v[52:55], v[180:183], v[144:147], v[52:55]\n\t"
    "v_perm_b32 v224, v201, v200, v220\n\t"
    "v_perm_b32 v225, v203, v202, v220\n\t"
    "v_mfma_f32_16x16x32_bf16 v[56:59], v[184:187], v[144:147], v[56:59]\n\t"
    "v_perm_b32 v226, v201, v200, v221\n\t"
    "v_perm_b32 v227, v203, v202, v221\n\t"
    "v_mfma_f32_16x16x32_bf16 v[48:51], v[232:235], v[148:151], v[48:51]\n\t"
    "v_perm_b32 v168, v225, v224, s65\n\t"
    "v_perm_b32 v169, v225, v224, s66\n\t"
    "ds_read_b128 v[144:147], v254 offset:24576\n\t"
    "v_mfma_f32_16x16x32_bf16 v[52:55], v[236:239], v[148:151], v[52:55]\n\t"
    "v_perm_b32 v170, v227, v226, s65\n\t"
    "v_perm_b32 v171, v227, v226, s66\n\t"
    "v_mfma_f32_16x16x32_bf16 v[56:59], v[240:243], v[148:151], v[56:59]\n\t"
    "s_waitcnt lgkmcnt(5)\n\t"
    "v_mfma_f32_16x16x32_bf16 v[32:35], v[160:163], v[152:155], v[32:35]\n\t"
    "v_add_u32 v216, s69, v248\n\t"
    "v_xor_b32 v217, 0x1000, v216\n\t"
    "ds_read_b128 v[148:151], v254 offset:40960\n\t"
    "v_mfma_f32_16x16x32_bf16 v[36:39], v[164:167], v[152:155], v[36:39]\n\t"
    "v_and_b32 v218, 0xffff, v251\n\t"
    "v_lshrrev_b32 v219, 16, v251\n\t"
    "v_mfma_f32_16x16x32_bf16 v[40:43], v[168:171], v[152:155], v[40:43]\n\t"
    "v_add_u32 v218, 131072, v218\n\t"
    "v_add_u32 v219, 131072, v219\n\t"
    "v_mfma_f32_16x16x32_bf16 v[0:3], v[160:163], v[156:159], v[0:3]\n\t"
    "ds_read_b128 v[152:155], v254 offset:57344\n\t"
    "v_mfma_f32_16x16x32_bf16 v[4:7], v[164:167], v[156:159], v[4:7]\n\t"
    "v_mfma_f32_16x16x32_bf16 v[8:11], v[168:171], v[156:159], v[8:11]\n\t"
    "ds_write_b128 v216, v[32:35] offset:0\n\t"
    "s_waitcnt lgkmcnt(6)\n\t"
    "v_mfma_f32_16x16x32_bf16 v[48:51], v[160:163], v[128:131], v[48:51]\n\t"
    "ds_read_b128 v[156:159], v253 offset:24576\n\t"
    "ds_write_b128 v216, v[36:39] offset:1024\n\t"
    "v_mfma_f32_16x16x32_bf16 v[52:55], v[164:167], v[128:131], v[52:55]\n\t"
    "ds_write_b128 v216, v[40:43] offset:2048\n\t"
    "v_mfma_f32_16x16x32_bf16 v[56:59], v[168:171], v[128:131], v[56:59]\n\t"
    "s_waitcnt lgkmcnt(0)\n\t"
    "s_barrier\n\t"
    "v_mfma_f32_16x16x32_bf16 v[16:19], v[176:179], v[132:135], v[16:19]\n\t"
    "ds_read_b128 v[128:131], v253 offset:40960\n\t"
    "s_cmp_lg_u32 %[wid], 0\n\t"
    "s_cbranch_scc1 .Lnoat_3_%=\n\t"
    "s_mov_b64 exec, 1\n\t"
    "v_mov_b32 v255, 0\n\t"
    "v_mov_b32 v220, 1\n\t"
    "global_atomic_add v255, v220, %[ctr] sc1\n\t"
    "s_mov_b64 exec, -1\n\t"
    "s_nop 3\n\t"
    ".Lnoat_3_%=:\n\t"
    "ds_read_b128 v[32:35], v217 offset:0\n\t"
    "v_mfma_f32_16x16x32_bf16 v[20:23], v[180:183], v[132:135], v[20:23]\n\t"
    "ds_read_b128 v[36:39], v217 offset:1024\n\t"
    "v_mfma_f32_16x16x32_bf16 v[24:27], v[184:187], v[132:135], v[24:27]\n\t"
    "ds_read_b128 v[40:43], v217 offset:2048\n\t"
    "s_waitcnt lgkmcnt(11)\n\t"
    "v_mfma_f32_16x16x32_bf16 v[16:19], v[232:235], v[136:139], v[16:19]\n\t"
    "s_waitcnt lgkmcnt(2)\n\t"
    "v_add_f32 v32, v0, v32\n\t"
    "v_add_f32 v33, v1, v33\n\t"
    "ds_read_b128 v[132:135], v253 offset:57344\n\t"
    "ds_write_b128 v216, v[48:51] offset:32768\n\t"
    "v_mfma_f32_16x16x32_bf16 v[20:23], v[236:239], v[136:139], v[20:23]\n\t"
    "v_add_f32 v34, v2, v34\n\t"
    "v_add_f32 v35, v3, v35\n\t"
    "ds_write_b128 v216, v[52:55] offset:33792\n\t"
    "v_mfma_f32_16x16x32_bf16 v[24:27], v[240:243], v[136:139], v[24:27]\n\t"
    "v_cvt_pk_bf16_f32 v32, v32, v33\n\t"
    "v_cvt_pk_bf16_f32 v33, v34, v35\n\t"
    "ds_write_b64 v218, v[32:33] offset:0\n\t"
    "s_waitcnt lgkmcnt(5)\n\t"
    "ds_write_b128 v216, v[56:59] offset:34816\n\t"
    "v_mfma_f32_16x16x32_bf16 v[16:19], v[160:163], v[140:143], v[16:19]\n\t"
    "v_add_f32 v36, v4, v36\n\t"
    "v_add_f32 v37, v5, v37\n\t"
    "ds_read_b128 v[136:139], v254 offset:26624\n\t"
    "v_mfma_f32_16x16x32_bf16 v[20:23], v[164:167], v[140:143], v[20:23]\n\t"
    "v_add_f32 v38, v6, v38\n\t"
    "v_add_f32 v39, v7, v39\n\t"
    "v_mfma_f32_16x16x32_bf16 v[24:27], v[168:171], v[140:143], v[24:27]\n\t"
    "v_cvt_pk_bf16_f32 v36, v36, v37\n\t"
    "v_cvt_pk_bf16_f32 v37, v38, v39\n\t"
    "ds_write_b64 v218, v[36:37] offset:32\n\t"
    "s_waitcnt lgkmcnt(7)\n\t"
    "s_waitcnt lgkmcnt(15)\n\t"
    "v_mfma_f32_16x16x32_bf16 v[96:99], v[176:179], v[144:147], v[96:99]\n\t"
    "v_add_f32 v40, v8, v40\n\t"
    "v_add_f32 v41, v9, v41\n\t"
    "ds_read_b128 v[140:143], v254 offset:43008\n\t"
    "v_mfma_f32_16x16x32_bf16 v[100:103], v[180:183], v[144:147], v[100:103]\n\t"
    "v_add_f32 v42, v10, v42\n\t"
    "v_add_f32 v43, v11, v43\n\t"
    "v_mfma_f32_16x16x32_bf16 v[104:107], v[184:187], v[144:147], v[104:107]\n\t"
    "v_cvt_pk_bf16_f32 v40, v40, v41\n\t"
    "v_cvt_pk_bf16_f32 v41, v42, v43\n\t"
    "ds_write_b64 v218, v[40:41] offset:64\n\t"
    "v_mfma_f32_16x16x32_bf16 v[96:99], v[232:235], v[148:151], v[96:99]\n\t"
    "ds_read_b128 v[144:147], v254 offset:59392\n\t"
    "s_waitcnt lgkmcnt(5)\n\t"
    "s_waitcnt lgkmcnt(1)\n\t"
    "s_barrier\n\t"
    "v_mfma_f32_16x16x32_bf16 v[100:103], v[236:239], v[148:151], v[100:103]\n\t"
    "ds_read_b128 v[224:227], v219 offset:0\n\t"
    "ds_read_b128 v[228:231], v219 offset:928\n\t"
    "v_mfma_f32_16x16x32_bf16 v[104:107], v[240:243], v[148:151], v[104:107]\n\t"
    "ds_read_b128 v[48:51], v217 offset:32768\n\t"
    "ds_read_b128 v[52:55], v217 offset:33792\n\t"
    "s_waitcnt lgkmcnt(15)\n\t"
    "v_mfma_f32_16x16x32_bf16 v[96:99], v[160:163], v[152:155], v[96:99]\n\t"
    "ds_read_b128 v[148:151], v253 offset:26624\n\t"
    "ds_read_b128 v[56:59], v217 offset:34816\n\t"
    "v_mfma_f32_16x16x32_bf16 v[100:103], v[164:167], v[152:155], v[100:103]\n\t"
    "s_waitcnt lgkmcnt(3)\n\t"
    "v_add_f32 v48, v16, v48\n\t"
    "v_add_f32 v49, v17, v49\n\t"
    "s_waitcnt lgkmcnt(5)\n\t"
    "s_add_u32 s71, s70, 0\n\t"
    "v_mfma_f32_16x16x32_bf16 v[104:107], v[168:171], v[152:155], v[104:107]\n\t"
    "v_add_f32 v50, v18, v50\n\t"
    "v_add_f32 v51, v19, v51\n\t"
    "buffer_store_dwordx4 v[224:227], v252, s[72:75], s71 offen sc1\n\t"
    "s_waitcnt lgkmcnt(4)\n\t"
    "v_mfma_f32_16x16x32_bf16 v[64:67], v[176:179], v[156:159], v[64:67]\n\t"
    "v_cvt_pk_bf16_f32 v48, v48, v49\n\t"
    "v_cvt_pk_bf16_f32 v49, v50, v51\n\t"
    "ds_write_b64 v218, v[48:49] offset:14848\n\t"
    "s_waitcnt lgkmcnt(3)\n\t"
    "ds_read_b128 v[152:155], v253 offset:43008\n\t"
    "s_add_u32 s71, s70, 14336\n\t"
    "buffer_store_dwordx4 v[228:231], v252, s[72:75], s71 offen sc1\n\t"
    "v_mfma_f32_16x16x32_bf16 v[68:71], v[180:183], v[156:159], v[68:71]\n\t"
    "v_add_f32 v52, v20, v52\n\t"
    "v_add_f32 v53, v21, v53\n\t"
    "v_mfma_f32_16x16x32_bf16 v[72:75], v[184:187], v[156:159], v[72:75]\n\t"
    "v_add_f32 v54, v22, v54\n\t"
    "v_add_f32 v55, v23, v55\n\t"
    "s_waitcnt lgkmcnt(15)\n\t"
    "v_mfma_f32_16x16x32_bf16 v[64:67], v[232:235], v[128:131], v[64:67]\n\t"
    "v_cvt_pk_bf16_f32 v52, v52, v53\n\t"
    "v_cvt_pk_bf16_f32 v53, v54, v55\n\t"
    "ds_write_b64 v218, v[52:53] offset:14880\n\t"
    "s_waitcnt lgkmcnt(3)\n\t"
    "ds_read_b128 v[156:159], v253 offset:59392\n\t"
    "ds_write_b128 v216, v[96:99] offset:0\n\t"
    "v_mfma_f32_16x16x32_bf16 v[68:71], v[236:239], v[128:131], v[68:71]\n\t"
    "v_add_f32 v56, v24, v56\n\t"
    "v_add_f32 v57, v25, v57\n\t"
    "ds_write_b128 v216, v[100:103] offset:1024\n\t"
    "v_mfma_f32_16x16x32_bf16 v[72:75], v[240:243], v[128:131], v[72:75]\n\t"
    "v_add_f32 v58, v26, v58\n\t"
    "v_add_f32 v59, v27, v59\n\t"
    "ds_write_b128 v216, v[104:107] offset:2048\n\t"
    "v_mfma_f32_16x16x32_bf16 v[64:67], v[160:163], v[132:135], v[64:67]\n\t"
    "v_cvt_pk_bf16_f32 v56, v56, v57\n\t"
    "v_cvt_pk_bf16_f32 v57, v58, v59\n\t"
    "ds_write_b64 v218, v[56:57] offset:14912\n\t"
    "v_mfma_f32_16x16x32_bf16 v[68:71], v[164:167], v[132:135], v[68:71]\n\t"
    "s_waitcnt lgkmcnt(1)\n\t"
    "s_waitcnt lgkmcnt(0)\n\t"
    "s_barrier\n\t"
    "v_mfma_f32_16x16x32_bf16 v[72:75], v[168:171], v[132:135], v[72:75]\n\t"
    "ds_read_b128 v[224:227], v219 offset:14848\n\t"
    "ds_read_b128 v[228:231], v219 offset:15776\n\t"
    "s_waitcnt lgkmcnt(15)\n\t"
    "v_mfma_f32_16x16x32_bf16 v[112:115], v[176:179], v[136:139], v[112:115]\n\t"
    "ds_read_b128 v[96:99], v217 offset:0\n\t"
    "ds_read_b128 v[100:103], v217 offset:1024\n\t"
    "v_mfma_f32_16x16x32_bf16 v[116:119], v[180:183], v[136:139], v[116:119]\n\t"
    "ds_read_b128 v[104:107], v217 offset:2048\n\t"
    "v_mfma_f32_16x16x32_bf16 v[120:123], v[184:187], v[136:139], v[120:123]\n\t"
    "s_waitcnt lgkmcnt(4)\n\t"
    "s_add_u32 s71, s70, 114688\n\t"
    "v_mfma_f32_16x16x32_bf16 v[112:115], v[232:235], v[140:143], v[112:115]\n\t"
    "s_waitcnt lgkmcnt(2)\n\t"
    "v_add_f32 v96, v64, v96\n\t"
    "v_add_f32 v97, v65, v97\n\t"
    "buffer_store_dwordx4 v[224:227], v252, s[72:75], s71 offen sc1\n\t"
    "s_waitcnt lgkmcnt(3)\n\t"
    "v_mfma_f32_16x16x32_bf16 v[116:119], v[236:239], v[140:143], v[116:119]\n\t"
    "v_add_f32 v98, v66, v98\n\t"
    "v_add_f32 v99, v67, v99\n\t"
    "s_add_u32 s71, s70, 129024\n\t"
    "buffer_store_dwordx4 v[228:231], v252, s[72:75], s71 offen sc1\n\t"
    "v_mfma_f32_16x16x32_bf16 v[120:123], v[240:243], v[140:143], v[120:123]\n\t"
    "v_cvt_pk_bf16_f32 v96, v96, v97\n\t"
    "v_cvt_pk_bf16_f32 v97, v98, v99\n\t"
    "ds_write_b64 v218, v[96:97] offset:0\n\t"
    "s_waitcnt lgkmcnt(2)\n\t"
    "s_waitcnt lgkmcnt(15)\n\t"
    "v_mfma_f32_16x16x32_bf16 v[112:115], v[160:163], v[144:147], v[112:115]\n\t"
    "v_add_f32 v100, v68, v100\n\t"
    "v_add_f32 v101, v69, v101\n\t"
    "v_mfma_f32_16x16x32_bf16 v[116:119], v[164:167], v[144:147], v[116:119]\n\t"
    "v_add_f32 v102, v70, v102\n\t"
    "v_add_f32 v103, v71, v103\n\t"
    "v_mfma_f32_16x16x32_bf16 v[120:123], v[168:171], v[144:147], v[120:123]\n\t"
    "v_cvt_pk_bf16_f32 v100, v100, v101\n\t"
    "v_cvt_pk_bf16_f32 v101, v102, v103\n\t"
    "ds_write_b64 v218, v[100:101] offset:32\n\t"
    "s_waitcnt lgkmcnt(2)\n\t"
    "v_mfma_f32_16x16x32_bf16 v[80:83], v[176:179], v[148:151], v[80:83]\n\t"
    "v_add_f32 v104, v72, v104\n\t"
    "v_add_f32 v105, v73, v105\n\t"
    "v_mfma_f32_16x16x32_bf16 v[84:87], v[180:183], v[148:151], v[84:87]\n\t"
    "v_add_f32 v106, v74, v106\n\t"
    "v_add_f32 v107, v75, v107\n\t"
    "v_mfma_f32_16x16x32_bf16 v[88:91], v[184:187], v[148:151], v[88:91]\n\t"
    "v_cvt_pk_bf16_f32 v104, v104, v105\n\t"
    "v_cvt_pk_bf16_f32 v105, v106, v107\n\t"
    "ds_write_b64 v218, v[104:105] offset:64\n\t"
    "ds_write_b128 v216, v[112:115] offset:32768\n\t"
    "s_waitcnt lgkmcnt(13)\n\t"
    "v_mfma_f32_16x16x32_bf16 v[80:83], v[232:235], v[152:155], v[80:83]\n\t"
    "ds_write_b128 v216, v[116:119] offset:33792\n\t"
    "v_mfma_f32_16x16x32_bf16 v[84:87], v[236:239], v[152:155], v[84:87]\n\t"
    "ds_write_b128 v216, v[120:123] offset:34816\n\t"
    "v_mfma_f32_16x16x32_bf16 v[88:91], v[240:243], v[152:155], v[88:91]\n\t"
    "s_waitcnt lgkmcnt(0)\n\t"
    "s_waitcnt lgkmcnt(3)\n\t"
    "s_barrier\n\t"
    "v_mfma_f32_16x16x32_bf16 v[80:83], v[160:163], v[156:159], v[80:83]\n\t"
    "ds_read_b128 v[224:227], v219 offset:0\n\t"
    "ds_read_b128 v[228:231], v219 offset:928\n\t"
    "v_mfma_f32_16x16x32_bf16 v[84:87], v[164:167], v[156:159], v[84:87]\n\t"
    "ds_read_b128 v[112:115], v217 offset:32768\n\t"
    "ds_read_b128 v[116:119], v217 offset:33792\n\t"
    "v_mfma_f32_16x16x32_bf16 v[88:91], v[168:171], v[156:159], v[88:91]\n\t"
    "ds_read_b128 v[120:123], v217 offset:34816\n\t"
    "s_setprio 0\n\t"
    "s_waitcnt lgkmcnt(4)\n\t"
    "s_add_u32 s71, s70, 458752\n\t"
    "buffer_store_dwordx4 v[224:227], v252, s[72:75], s71 offen sc1\n\t"
    "s_waitcnt lgkmcnt(3)\n\t"
    "s_add_u32 s71, s70, 473088\n\t"
    "buffer_store_dwordx4 v[228:231], v252, s[72:75], s71 offen sc1\n\t"
    "s_nop 3\n\t"
    "s_waitcnt lgkmcnt(2)\n\t"
    "v_add_f32 v112, v80, v112\n\t"
    "v_add_f32 v113, v81, v113\n\t"
    "v_add_f32 v114, v82, v114\n\t"
    "v_add_f32 v115, v83, v115\n\t"
    "v_cvt_pk_bf16_f32 v112, v112, v113\n\t"
    "v_cvt_pk_bf16_f32 v113, v114, v115\n\t"
    "ds_write_b64 v218, v[112:113] offset:14848\n\t"
    "s_waitcnt lgkmcnt(2)\n\t"
    "v_add_f32 v116, v84, v116\n\t"
    "v_add_f32 v117, v85, v117\n\t"
    "v_add_f32 v118, v86, v118\n\t"
    "v_add_f32 v119, v87, v119\n\t"
    "v_cvt_pk_bf16_f32 v116, v116, v117\n\t"
    "v_cvt_pk_bf16_f32 v117, v118, v119\n\t"
    "ds_write_b64 v218, v[116:117] offset:14880\n\t"
    "s_waitcnt lgkmcnt(2)\n\t"
    "v_add_f32 v120, v88, v120\n\t"
    "v_add_f32 v121, v89, v121\n\t"
    "v_add_f32 v122, v90, v122\n\t"
    "v_add_f32 v123, v91, v123\n\t"
    "v_cvt_pk_bf16_f32 v120, v120, v121\n\t"
    "v_cvt_pk_bf16_f32 v121, v122, v123\n\t"
    "ds_write_b64 v218, v[120:121] offset:14912\n\t"
    "s_waitcnt lgkmcnt(0)\n\t"
    "s_barrier\n\t"
    "ds_read_b128 v[224:227], v219 offset:14848\n\t"
    "ds_read_b128 v[228:231], v219 offset:15776\n\t"
    "s_waitcnt lgkmcnt(1)\n\t"
    "s_add_u32 s71, s70, 573440\n\t"
    "buffer_store_dwordx4 v[224:227], v252, s[72:75], s71 offen sc1\n\t"
    "s_waitcnt lgkmcnt(0)\n\t"
    "s_add_u32 s71, s70, 587776\n\t"
    "buffer_store_dwordx4 v[228:231], v252, s[72:75], s71 offen sc1\n\t"
    "s_branch .Lend_3_%=\n\t"
    ".Lslow0_3_%=:\n\t"
    "s_add_i32 s71, s59, -2048\n\t"
    "s_movk_i32 s70, 0x7fff\n\t"
    ".Lretry0_3_%=:\n\t"
    "s_sleep 1\n\t"
    "buffer_load_dwordx4 v[212:215], v248, s[48:51], s71 offen sc1\n\t"
    "s_waitcnt vmcnt(0)\n\t"
    "v_bitop3_b32 v255, v215, v250, s64 bitop3:0x28\n\t"
    "v_cmp_ne_u32 vcc, 0, v255\n\t"
    "s_cbranch_vccz .Lback0_3_%=\n\t"
    "s_sub_u32 s70, s70, 1\n\t"
    "s_cbranch_scc0 .Lretry0_3_%=\n\t"
    "s_branch .Lback0_3_%=\n\t"
    ".Lslow1_3_%=:\n\t"
    "s_add_i32 s71, s59, -4096\n\t"
    "s_movk_i32 s70, 0x7fff\n\t"
    ".Lretry1_3_%=:\n\t"
    "s_sleep 1\n\t"
    "buffer_load_dwordx4 v[208:211], v248, s[48:51], s71 offen sc1\n\t"
    "s_waitcnt vmcnt(0)\n\t"
    "v_bitop3_b32 v255, v211, v249, s64 bitop3:0x28\n\t"
    "v_cmp_ne_u32 vcc, 0, v255\n\t"
    "s_cbranch_vccz .Lback1_3_%=\n\t"
    "s_sub_u32 s70, s70, 1\n\t"
    "s_cbranch_scc0 .Lretry1_3_%=\n\t"
    "s_branch .Lback1_3_%=\n\t"
    ".Lslowa_3_%=:\n\t"
    "s_add_i32 s71, s59, -2048\n\t"
    "s_movk_i32 s57, 0x7fff\n\t"
    ".Lretrya_3_%=:\n\t"
    "s_sleep 1\n\t"
    "buffer_load_dwordx4 v[212:215], v248, s[48:51], s71 offen sc1\n\t"
    "s_waitcnt vmcnt(0)\n\t"
    "v_bitop3_b32 v255, v215, v250, s64 bitop3:0x28\n\t"
    "v_cmp_ne_u32 vcc, 0, v255\n\t"
    "s_cbranch_vccz .Lbacka_3_%=\n\t"
    "s_sub_u32 s57, s57, 1\n\t"
    "s_cbranch_scc0 .Lretrya_3_%=\n\t"
    "s_branch .Lbacka_3_%=\n\t"
    ".Lslowb_3_%=:\n\t"
    "s_add_i32 s71, s59, 0\n\t"
    "s_movk_i32 s57, 0x7fff\n\t"
    ".Lretryb_3_%=:\n\t"
    "s_sleep 1\n\t"
    "buffer_load_dwordx4 v[208:211], v248, s[48:51], s71 offen sc1\n\t"
    "s_waitcnt vmcnt(0)\n\t"
    "v_bitop3_b32 v255, v211, v249, s64 bitop3:0x28\n\t"
    "v_cmp_ne_u32 vcc, 0, v255\n\t"
    "s_cbranch_vccz .Lbackb_3_%=\n\t"
    "s_sub_u32 s57, s57, 1\n\t"
    "s_cbranch_scc0 .Lretryb_3_%=\n\t"
    "s_branch .Lbackb_3_%=\n\t"
    ".Lslowc_3_%=:\n\t"
    "s_add_i32 s71, s59, 2048\n\t"
    "s_movk_i32 s57, 0x7fff\n\t"
    ".Lretryc_3_%=:\n\t"
    "s_sleep 1\n\t"
    "buffer_load_dwordx4 v[212:215], v248, s[48:51], s71 offen sc1\n\t"
    "s_waitcnt vmcnt(0)\n\t"
    "v_bitop3_b32 v255, v215, v250, s64 bitop3:0x28\n\t"
    "v_cmp_ne_u32 vcc, 0, v255\n\t"
    "s_cbranch_vccz .Lbackc_3_%=\n\t"
    "s_sub_u32 s57, s57, 1\n\t"
    "s_cbranch_scc0 .Lretryc_3_%=\n\t"
    "s_branch .Lbackc_3_%=\n\t"
    ".Lend_3_%=:\n\t"
    ".Lalldone_%=:\n\t"
        : "+{v[0:31]}"(A.a[0]), "+{v[32:63]}"(A.a[1]), "+{v[64:95]}"(A.a[2]), "+{v[96:127]}"(A.a[3]), "+{v[208:215]}"(C), "+{v[232:247]}"(MI),
          "+{v[248:255]}"(MJ)
        : "{v[192:207]}"(L), "{s[40:43]}"(xr), "{s[44:47]}"(qr), "{s[48:51]}"(rrd), "{s[52:55]}"(sr), "{s[72:75]}"(orr),
          [xk] "s"(x_lds | 0x10000u), [ql] "s"(q_lds), [so] "s"((unsigned)((12 + t_ksl) * 1024)), [hh] "s"(hh), [wid] "s"(wid), [ctr] "s"(ctr)
        : "v128", "v129", "v130", "v131", "v132", "v133", "v134", "v135", "v136", "v137", "v138", "v139", "v140", "v141", "v142", "v143", "v144", "v145", "v146", "v147", "v148", "v149", "v150", "v151", "v152", "v153", "v154", "v155", "v156", "v157", "v158", "v159", "v160", "v161", "v162", "v163", "v164", "v165", "v166", "v167", "v168", "v169", "v170", "v171", "v172", "v173", "v174", "v175", "v176", "v177", "v178", "v179", "v180", "v181", "v182", "v183", "v184", "v185", "v186", "v187", "v188", "v189", "v190", "v191", "v216", "v217", "v218", "v219", "v220", "v221", "v222", "v223", "v224", "v225", "v226", "v227", "v228", "v229", "v230", "v231", "s56", "s57", "s58", "s59", "s60", "s61", "s62", "s63", "s64", "s65", "s66", "s67", "s68", "s69", "s70", "s71", "vcc", "scc", "memory");
}

}

extern "C" void kernel_launch(void* const* d_in, const int* in_sizes, int n_in, void* d_out, int out_size, void* d_ws, size_t ws_size,
                              hipStream_t stream) {
    const __hip_bfloat16* x = (const __hip_bfloat16*)d_in[0];
    const int* qw = (const int*)d_in[1];
    const __hip_bfloat16* lut = (const __hip_bfloat16*)d_in[2];
    __hip_bfloat16* out = (__hip_bfloat16*)d_out;
    sqllm_fused_kernel<<<dim3(256), dim3(512), 0, stream>>>(x, qw, lut, out, (char*)d_ws);
}
```
